```python
import torch

def _softmax(x, axis=-1):
    return torch.softmax(x, dim=axis)

def _gelu(x):
    return torch.nn.functional.gelu(x, approximate='tanh')

def _silu(x):
    return torch.nn.functional.silu(x)

def _sigmoid(x):
    return torch.sigmoid(x)

def _layernorm(x, axis=-1, eps=1e-05):
    m = x.mean(dim=axis, keepdim=True)
    v = x.var(dim=axis, keepdim=True, unbiased=False)
    return (x - m) / torch.sqrt(v + eps)

def _scatter_add(tgt, idx, src, axis=0):
    return tgt.clone().index_add_(axis, idx, src)

def _segment_sum(src, seg, n):
    out = torch.zeros((n,) + src.shape[1:], dtype=src.dtype)
    return out.index_add_(0, seg, src)

def _pad(x, pw):
    flat = tuple((v for pair in reversed(pw) for v in pair))
    return torch.nn.functional.pad(x, flat)
kernel_name = 'parallel_sum__memory_aug_recurrent_mem_tr__swiglu_ffn__prompt'

def setup_inputs():

    _w = lambda a: (lambda t: t.to(torch.bfloat16) if t.is_floating_point() else t)(torch.as_tensor(a))

    def _A_setup():
        B, S, Dm = (1, 2048, 512)
        H, Dh = (32, 128)
        M, seg = (8, 32)
        seg = min(seg, S)
        S = S // seg * seg
        return {'x': _w((lambda *s: torch.randn(*s, dtype=torch.bfloat16) * 0.05)(B, S, Dm)), 'mem0': _w((lambda *s: torch.randn(*s, dtype=torch.bfloat16) * 1.6)(B, M, Dm)), 'Wq': _w((lambda *s: torch.randn(*s, dtype=torch.bfloat16) * 0.8)(Dm, H, Dh) * Dm ** (-0.5)), 'Wk': _w((lambda *s: torch.randn(*s, dtype=torch.bfloat16) * 0.8)(Dm, H, Dh) * Dm ** (-0.5)), 'Wv': _w((lambda *s: torch.randn(*s, dtype=torch.bfloat16) * 1.6)(Dm, H, Dh) * Dm ** (-0.5)), 'Wo': _w((lambda *s: torch.randn(*s, dtype=torch.bfloat16) * 1.6)(H, Dh, Dm) * (H * Dh) ** (-0.5)), 'seg_len': seg}
    _A_in = _A_setup()

    def _B_setup():
        B, S, D, F = (1, 2048, 512, 1792)
        return {'x': _w((lambda *s: torch.randn(*s, dtype=torch.bfloat16) * 0.05)(B, S, D)), 'w_gate': _w((lambda *s: torch.randn(*s, dtype=torch.bfloat16) * 0.05)(D, F)), 'w_up': _w((lambda *s: torch.randn(*s, dtype=torch.bfloat16) * 0.05)(D, F)), 'w_down': _w((lambda *s: torch.randn(*s, dtype=torch.bfloat16) * 0.05)(F, D))}
    _B_in = _B_setup()
    return {'A_x': _A_in['x'], 'A_mem0': _A_in['mem0'], 'A_Wq': _A_in['Wq'], 'A_Wk': _A_in['Wk'], 'A_Wv': _A_in['Wv'], 'A_Wo': _A_in['Wo'], 'A_seg_len': _A_in['seg_len'], 'B_x': _B_in['x'], 'B_w_gate': _B_in['w_gate'], 'B_w_up': _B_in['w_up'], 'B_w_down': _B_in['w_down']}

def reference(A_x, A_mem0, A_Wq, A_Wk, A_Wv, A_Wo, A_seg_len, B_x, B_w_gate, B_w_up, B_w_down):

    def _A_ref(x, mem0, Wq, Wk, Wv, Wo, seg_len):
        B, S, Dm = x.shape
        H, Dh = (Wq.shape[1], Wq.shape[2])
        M = mem0.shape[1]
        scale = Dh ** (-0.5)
        n_seg = S // seg_len
        L = M + seg_len
        idx = torch.arange(L)
        mask = (idx[:, None] < M) | (idx[:, None] >= idx[None, :])
        mem = mem0
        outs = []
        for s in range(n_seg):
            xs = x[:, s * seg_len:(s + 1) * seg_len, :].clone()
            h = torch.concatenate([mem, xs], axis=1)
            q = torch.einsum('btd,dhe->bhte', h, Wq)
            k = torch.einsum('btd,dhe->bhte', h, Wk)
            v = torch.einsum('btd,dhe->bhte', h, Wv)
            scores = torch.einsum('bhqe,bhke->bhqk', q, k) * scale
            scores = torch.where(mask[None, None], scores, -1000000000.0)
            p = _softmax(scores, axis=-1)
            o = torch.einsum('bhqk,bhke->bhqe', p, v)
            o = torch.einsum('bhte,hed->btd', o, Wo)
            mem = o[:, :M, :].clone()
            outs.append(o[:, M:, :])
        return torch.concatenate(outs, axis=1)

    def _B_ref(x, w_gate, w_up, w_down):
        g = _silu(x @ w_gate)
        u = x @ w_up
        return g * u @ w_down
    x = A_x
    xn = _layernorm(x, axis=-1)
    return x + _A_ref(xn, A_mem0, A_Wq, A_Wk, A_Wv, A_Wo, A_seg_len) + _B_ref(xn, B_w_gate, B_w_up, B_w_down)
```

```cpp
#include <hip/hip_runtime.h>
#include <hip/hip_bf16.h>
typedef unsigned short u16;
__device__ __forceinline__ float b2f(u16 x) { return __uint_as_float((unsigned)x << 16); }
__device__ __forceinline__ u16 f2b(float f) { __bf16 b = (__bf16)f; return __builtin_bit_cast(u16, b); }
__device__ __forceinline__ float rb(float f) { return b2f(f2b(f)); }

__global__ void k_ln(const u16* x, u16* xn) {
    int row = blockIdx.x, t = threadIdx.x;
    float v[8]; float s = 0;
    for (int j = 0; j < 8; ++j) { v[j] = b2f(x[row * 512 + t * 8 + j]); s += v[j]; }
    for (int m = 32; m >= 1; m >>= 1) s += __shfl_xor(s, m);
    float mean = s / 512.f; float q = 0;
    for (int j = 0; j < 8; ++j) q += (v[j] - mean) * (v[j] - mean);
    for (int m = 32; m >= 1; m >>= 1) q += __shfl_xor(q, m);
    float var = rb(q / 512.f); float mb = rb(mean);
    float sd = rb(sqrtf(rb(var + 1e-5f)));
    for (int j = 0; j < 8; ++j) xn[row * 512 + t * 8 + j] = f2b(rb(v[j] - mb) / sd);
}
template <int OUTF32>
__global__ void k_gemm(const u16* A, const u16* B, void* C, int M, int N, int K, int ldc) {
    __shared__ float As[16][65], Bs[16][65];
    int tx = threadIdx.x & 15, ty = threadIdx.x >> 4;
    int m0 = blockIdx.y * 64, n0 = blockIdx.x * 64;
    float acc[4][4] = {};
    for (int k0 = 0; k0 < K; k0 += 16) {
        for (int i = threadIdx.x; i < 1024; i += 256) {
            int r = i >> 4, c = i & 15; As[c][r] = (m0 + r < M) ? b2f(A[(size_t)(m0 + r) * K + k0 + c]) : 0.f;
            int kr = i >> 6, nc = i & 63; Bs[kr][nc] = b2f(B[(size_t)(k0 + kr) * N + n0 + nc]);
        }
        __syncthreads();
        for (int k = 0; k < 16; ++k) for (int i = 0; i < 4; ++i) for (int j = 0; j < 4; ++j) acc[i][j] += As[k][ty * 4 + i] * Bs[k][tx * 4 + j];
        __syncthreads();
    }
    for (int i = 0; i < 4; ++i) for (int j = 0; j < 4; ++j) { int r = m0 + ty * 4 + i; if (r < M) { if (OUTF32) ((float*)C)[(size_t)r * ldc + n0 + tx * 4 + j] = acc[i][j]; else ((u16*)C)[(size_t)r * ldc + n0 + tx * 4 + j] = f2b(acc[i][j]); } }
}
__global__ void k_attn(const u16* qm, const u16* km, const u16* vm, const u16* Q, const u16* K, const u16* V, u16* om, u16* O, int s0, int mem) {
    int h = blockIdx.x, s = s0 + blockIdx.y, i = blockIdx.z, t = threadIdx.x;
    __shared__ float p[40];
    const u16* q = mem ? qm + (size_t)(s * 8 + i) * 4096 + h * 128 : Q + (size_t)(s * 32 + i) * 4096 + h * 128;
    if (t < 40) {
        const u16* k = t < 8 ? km + (size_t)(s * 8 + t) * 4096 + h * 128 : K + (size_t)(s * 32 + t - 8) * 4096 + h * 128;
        float a = 0; for (int e = 0; e < 128; ++e) a += b2f(q[e]) * b2f(k[e]);
        a = rb(rb(a) * rb(0.08838834764831845f));
        if (!mem && t - 8 > i) a = -1e9f;
        p[t] = a;
    }
    __syncthreads();
    float mx = -1e30f; for (int j = 0; j < 40; ++j) mx = fmaxf(mx, p[j]);
    float sum = 0; for (int j = 0; j < 40; ++j) sum += expf(p[j] - mx);
    float o = 0;
    for (int j = 0; j < 40; ++j) { const u16* v = j < 8 ? vm + (size_t)(s * 8 + j) * 4096 + h * 128 : V + (size_t)(s * 32 + j - 8) * 4096 + h * 128; o += rb(expf(p[j] - mx) / sum) * b2f(v[t]); }
    if (mem) om[(size_t)i * 4096 + h * 128 + t] = f2b(o); else O[(size_t)(s * 32 + i) * 4096 + h * 128 + t] = f2b(o);
}
__global__ void k_swiglu(const u16* g, const u16* u, u16* hh, int n) { int i = blockIdx.x * 256 + threadIdx.x; if (i < n) { float a = b2f(g[i]); float s = rb(a / (1.f + expf(-a))); hh[i] = f2b(s * b2f(u[i])); } }
__global__ void k_final(const u16* x, const u16* a, const u16* b, u16* out, int n) { int i = blockIdx.x * 256 + threadIdx.x; if (i < n) out[i] = f2b(rb(b2f(x[i]) + b2f(a[i])) + b2f(b[i])); }

extern "C" void kernel_launch(void* const* d_in, const int* in_sizes, int n_in, void* d_out, int out_size, void* d_ws, size_t ws_size, hipStream_t st) {
    const u16 *x = (const u16*)d_in[0], *mem0 = (const u16*)d_in[1], *Wq = (const u16*)d_in[2], *Wk = (const u16*)d_in[3], *Wv = (const u16*)d_in[4], *Wo = (const u16*)d_in[5];
    const u16 *wg = (const u16*)d_in[8], *wu = (const u16*)d_in[9], *wd = (const u16*)d_in[10];
    u16* w = (u16*)d_ws; size_t o = 0;
    u16* xn = w + o; o += 2048 * 512; u16* Q = w + o; o += 2048 * 4096; u16* K = w + o; o += 2048 * 4096; u16* V = w + o; o += 2048 * 4096; u16* O = w + o; o += 2048 * 4096;
    u16* qm = w + o; o += 512 * 4096; u16* km = w + o; o += 512 * 4096; u16* vm = w + o; o += 512 * 4096; u16* om = w + o; o += 8 * 4096; u16* mem = w + o; o += 65 * 8 * 512;
    u16* g = w + o; o += 2048 * 1792; u16* u = w + o; o += 2048 * 1792; u16* hh = w + o; o += 2048 * 1792; u16* A = w + o; o += 2048 * 512; u16* B = w + o; o += 2048 * 512;
    k_ln<<<2048, 64, 0, st>>>(x, xn);
    k_gemm<0><<<dim3(64, 32), 256, 0, st>>>(xn, Wq, Q, 2048, 4096, 512, 4096);
    k_gemm<0><<<dim3(64, 32), 256, 0, st>>>(xn, Wk, K, 2048, 4096, 512, 4096);
    k_gemm<0><<<dim3(64, 32), 256, 0, st>>>(xn, Wv, V, 2048, 4096, 512, 4096);
    hipMemcpyAsync(mem, mem0, 8 * 512 * 2, hipMemcpyDeviceToDevice, st);
    for (int s = 0; s < 64; ++s) {
        k_gemm<0><<<dim3(64, 1), 256, 0, st>>>(mem + s * 4096, Wq, qm + (size_t)s * 8 * 4096, 8, 4096, 512, 4096);
        k_gemm<0><<<dim3(64, 1), 256, 0, st>>>(mem + s * 4096, Wk, km + (size_t)s * 8 * 4096, 8, 4096, 512, 4096);
        k_gemm<0><<<dim3(64, 1), 256, 0, st>>>(mem + s * 4096, Wv, vm + (size_t)s * 8 * 4096, 8, 4096, 512, 4096);
        k_attn<<<dim3(32, 1, 8), 128, 0, st>>>(qm, km, vm, Q, K, V, om, O, s, 1);
        k_gemm<0><<<dim3(8, 1), 256, 0, st>>>(om, Wo, mem + (s + 1) * 4096, 8, 512, 4096, 512);
    }
    k_attn<<<dim3(32, 64, 32), 128, 0, st>>>(qm, km, vm, Q, K, V, om, O, 0, 0);
    k_gemm<0><<<dim3(8, 32), 256, 0, st>>>(O, Wo, A, 2048, 512, 4096, 512);
    k_gemm<0><<<dim3(28, 32), 256, 0, st>>>(xn, wg, g, 2048, 1792, 512, 1792);
    k_gemm<0><<<dim3(28, 32), 256, 0, st>>>(xn, wu, u, 2048, 1792, 512, 1792);
    k_swiglu<<<2048 * 1792 / 256, 256, 0, st>>>(g, u, hh, 2048 * 1792);
    k_gemm<0><<<dim3(8, 32), 256, 0, st>>>(hh, wd, B, 2048, 512, 1792, 512);
    k_final<<<2048 * 512 / 256, 256, 0, st>>>(x, A, B, (u16*)d_out, 2048 * 512);
}
```

```cpp
#define NX 4
#define NOWN ((8 - NX) * 4)
#define TOUCH 1
#define CFLIP 0
#include <hip/hip_runtime.h>
#include <hip/hip_bf16.h>
typedef unsigned short u16;
typedef unsigned v4u __attribute__((ext_vector_type(4)));
typedef unsigned v2u __attribute__((ext_vector_type(2)));
typedef float v4f __attribute__((ext_vector_type(4)));
typedef float v2f __attribute__((ext_vector_type(2)));
typedef short s16x4 __attribute__((ext_vector_type(4)));
typedef __bf16 bf16x2 __attribute__((ext_vector_type(2)));
typedef __bf16 bf16x8 __attribute__((ext_vector_type(8)));
typedef unsigned v8u __attribute__((ext_vector_type(8)));
typedef unsigned v16u __attribute__((ext_vector_type(16)));
#define LAS __attribute__((address_space(3)))
#define GAS __attribute__((address_space(1)))
#define DEVI __device__ __forceinline__
#define RFL(x) ((unsigned)__builtin_amdgcn_readfirstlane((int)(x)))
#define RLX_AGENT __ATOMIC_RELAXED, __HIP_MEMORY_SCOPE_AGENT

DEVI float b2f(u16 x) { return __uint_as_float((unsigned)x << 16); }
DEVI u16 f2b(float f) { __bf16 b = (__bf16)f; return __builtin_bit_cast(u16, b); }
DEVI float rb(float f) { return b2f(f2b(f)); }
DEVI unsigned pk2(float a, float b) { v2f v = {a, b}; bf16x2 r = __builtin_convertvector(v, bf16x2); return __builtin_bit_cast(unsigned, r); }
DEVI v2u trread(unsigned addr) { return __builtin_bit_cast(v2u, __builtin_amdgcn_ds_read_tr16_b64_v4i16((LAS s16x4*)(size_t)addr)); }
DEVI v4u lds128(unsigned addr) { return *(LAS v4u*)(size_t)addr; }
DEVI void sts128(unsigned addr, v4u v) { *(LAS v4u*)(size_t)addr = v; }
DEVI v4f mf32(v4u a, v4u b, v4f c) { return __builtin_amdgcn_mfma_f32_16x16x32_bf16(__builtin_bit_cast(bf16x8, a), __builtin_bit_cast(bf16x8, b), c, 0, 0, 0); }
DEVI v4f mf16(v2u a, v2u b, v4f c) { return __builtin_amdgcn_mfma_f32_16x16x16bf16_1k(__builtin_bit_cast(s16x4, a), __builtin_bit_cast(s16x4, b), c, 0, 0, 0); }
DEVI unsigned clk() { unsigned long long t; asm volatile("s_memtime %0\n\ts_waitcnt lgkmcnt(0)" : "=s"(t) :: "memory"); return (unsigned)t; }
DEVI unsigned rtc() { unsigned long long t; asm volatile("s_memrealtime %0\n\ts_waitcnt lgkmcnt(0)" : "=s"(t) :: "memory"); return (unsigned)t; }

DEVI void st16(v4u v, __amdgpu_buffer_rsrc_t r, unsigned vo, unsigned so) { __builtin_amdgcn_raw_buffer_store_b128(v, r, vo, so, 16); asm volatile("s_nop 1" :: "v"(v) : "memory"); }
constexpr size_t MB = 1u << 20;
constexpr unsigned WS_CTL = 0;
constexpr unsigned WS_M0 = 128 * 1024;
constexpr unsigned WS_XN = 1 * MB;
constexpr unsigned WS_SCR = 3 * MB;
constexpr unsigned WS_COMM = 4 * MB;
constexpr unsigned WS_QF = 0, WS_KF = 16 * MB, WS_KM = 48 * MB, WS_VM = 52 * MB, WS_H = 56 * MB, WS_PB = 64 * MB;
DEVI unsigned wspread(unsigned o) { return o + (o & ~0x1FFFu); }
DEVI unsigned wmap(unsigned o, unsigned par13) { const unsigned q = wspread(o); return q | (((q >> 8) & 0x2000u) ^ par13); }
constexpr unsigned CT_EPOCH = 0, CT_ROT = 1024, CT_GO = 2048, CT_INIT = 20480;
constexpr unsigned POLL_CAP = 1u << 18;
DEVI unsigned comm_offset(char* ws) { return (unsigned)((((size_t)ws + WS_COMM + (2u << 20) - 1) & ~(size_t)((2u << 20) - 1)) - (size_t)ws); }
DEVI unsigned wait_go(char* ws, int b7, unsigned long long tag) {
    unsigned long long v; unsigned n = 0;
    for (;;) { v = __hip_atomic_load((GAS unsigned long long*)(ws + CT_GO + b7 * 128), RLX_AGENT); if ((v & ~0xFFull) == tag || ++n > POLL_CAP) break; __builtin_amdgcn_s_sleep(3); }
    asm volatile("" ::: "memory");
    return RFL((unsigned)v & 63u);
}
constexpr unsigned SENT = 0xFFFFFFFFu;
__host__ __device__ constexpr int kperm(int g, int a) { return 16 * a + 4 * g; }

constexpr unsigned CT_UNIT = 65536;
typedef float v16f __attribute__((ext_vector_type(16)));
DEVI void mf_wx(v16f& c, const v4u& w, const v4u& x) { asm volatile("v_mfma_f32_32x32x16_bf16 %0, %1, %2, %0" : "+v"(c) : "a"(w), "v"(x)); }
DEVI void mf_xw(v16f& c, const v4u& w, const v4u& x) { asm volatile("v_mfma_f32_32x32x16_bf16 %0, %2, %1, %0" : "+v"(c) : "a"(w), "v"(x)); }
DEVI void mf_wx0(v16f& c, const v4u& w, const v4u& x) { asm volatile("v_mfma_f32_32x32x16_bf16 %0, %1, %2, 0" : "=&v"(c) : "a"(w), "v"(x)); }
DEVI void mf_xw0(v16f& c, const v4u& w, const v4u& x) { asm volatile("v_mfma_f32_32x32x16_bf16 %0, %2, %1, 0" : "=&v"(c) : "a"(w), "v"(x)); }
DEVI void glds16(const char* g, unsigned lds) { asm volatile("s_mov_b32 m0, %1\n\ts_nop 0\n\tglobal_load_lds_dwordx4 %0, off" :: "v"(g), "s"(lds)); }
template <int OFF> DEVI void blds_n(unsigned vo, v4u srd, unsigned so) { asm volatile("buffer_load_dwordx4 %0, %1, %2 offen offset:%3 sc1 lds" :: "v"(vo), "s"(srd), "s"(so), "i"(OFF)); }
template <int OFF> DEVI void blds(unsigned vo, v4u srd, unsigned so, unsigned lds) { asm volatile("s_mov_b32 m0, %3\n\ts_nop 0\n\tbuffer_load_dwordx4 %0, %1, %2 offen offset:%4 sc1 lds" :: "v"(vo), "s"(srd), "s"(so), "s"(lds), "i"(OFF)); }
DEVI v4u pack8(const v4f& a, const v4f& b) { return v4u{pk2(a[0], a[1]), pk2(a[2], a[3]), pk2(b[0], b[1]), pk2(b[2], b[3])}; }
DEVI float swiglu(float g, float u) { return g * u * __builtin_amdgcn_rcpf(1.f + __builtin_amdgcn_exp2f(-1.4426950408889634f * g)); }

template <int TYPE>
DEVI unsigned phaseA_run(size_t lo, size_t hi, char* ws, char* wide, unsigned go, int b7, int l, int w, int type, int quad, int par) {
    const int g = l >> 4, q4 = (l >> 2) & 3, p4 = l & 3;
    const int idx = quad * 4 + w;
    const unsigned l16 = l * 16;
    const v4u srd = {RFL((size_t)ws), RFL((size_t)ws >> 32) & 0xffffu, 0x10000000u, 0x20000u};
    const __amdgpu_buffer_rsrc_t rW = __builtin_amdgcn_make_buffer_rsrc(wide, 0, 0x9400000, 0x00020000);
    v4u wf[16][4];
    {
        constexpr bool f = TYPE == 3;
        constexpr unsigned stride = f ? 3584 : 8192;
        const int row = l >> 3, ch = (l & 7) ^ (((row >> 1) & 3) << 1);
        const char* src = (const char*)((ch < 4 ? lo : hi) + (size_t)(row * stride + (idx << (f ? 6 : 7)) + ch * 16));
        const int hf = g >> 1, Q = 4 * (g & 1) + p4, cq = TYPE == 1 ? Q : ((Q & 1) | ((Q & 2) << 1) | ((Q & 4) >> 1));
        const unsigned b0 = w * 32768 + (4 * hf + q4) * 128 + ((((cq >> 2) ^ (2 * hf + (q4 >> 1))) & 3) * 32) + (cq & 3) * 8;
#pragma unroll 4
        for (int i = 0; i < 32; ++i) { glds16(src, w * 32768 + i * 1024); src += (size_t)stride * 8; }
#pragma unroll
        for (int r = 0; r < 4; ++r) {
            if (r < 3) asm volatile("s_waitcnt vmcnt(16)" ::: "memory"); else asm volatile("s_waitcnt vmcnt(0)" ::: "memory");
#pragma unroll
            for (int kk = 0; kk < 4; ++kk)
#pragma unroll
                for (int ct = 0; ct < 4; ++ct) {
                    const unsigned a = (b0 ^ ((ct & 1) * 64)) + (ct >> 1) * 1024 + (r & 1) * 16384 + kk * 4096;
                    v2u x = trread(a), y = trread(a + 2048); wf[4 * r + kk][ct] = v4u{x.x, x.y, y.x, y.y};
                }
#pragma unroll
            for (int kk = 0; kk < 4; ++kk) asm volatile("" : "+a"(wf[4 * r + kk][0]), "+a"(wf[4 * r + kk][1]), "+a"(wf[4 * r + kk][2]), "+a"(wf[4 * r + kk][3]) :: "memory");
            if (r < 2) {
#pragma unroll 4
                for (int i = 0; i < 16; ++i) { glds16(src, w * 32768 + (r & 1) * 16384 + i * 1024); src += (size_t)stride * 8; }
            }
        }
    }
    const unsigned obase = type == 0 ? WS_KF : WS_QF, osh = type == 0 ? 12 : 11;
    const unsigned flag = CT_UNIT + (type * 64 + idx) * 128 + par * 4;
    constexpr int NH = 4;
    const int nh = type < 2 ? NH : 0, nit = par ? NH : 64 - nh;
    auto seg = [&](int it) { return it < nh ? 2 * it + par : min(it + nh, 64); };
    auto xsrc = [&](int it) { return WS_XN + (unsigned)min(seg(it), 63) * 32768u + w * 8192; };
#define AST(k)
#define ACK(k)
    AST(0);
    if (type < 2) {
        const unsigned long long e = __hip_atomic_load((GAS unsigned long long*)(ws + CT_EPOCH), RLX_AGENT);
        const unsigned g2 = wait_go(ws, b7, (e & ~0xFFull) + 0x100);
        if ((g2 >> 3) != (go >> 3)) return g2;
        go = g2;
    }
    const unsigned p13 = (go & 2) << 12;
    AST(1);
    asm volatile("s_barrier" ::: "memory");
#pragma unroll 1
    for (int i = 0; i < 3; ++i) {
        const unsigned so = xsrc(i), d = i * 32768 + w * 8192;
        blds<0>(l16, srd, so, d); blds<1024>(l16, srd, so, d); blds<2048>(l16, srd, so, d); blds<3072>(l16, srd, so, d);
        blds<0>(l16, srd, so + 4096, d + 4096); blds<1024>(l16, srd, so + 4096, d + 4096); blds<2048>(l16, srd, so + 4096, d + 4096); blds<3072>(l16, srd, so + 4096, d + 4096);
    }
    constexpr int NS = TYPE == 3 ? 2 : 4;
    const unsigned ov = (g & 1) * 1024 + (g >> 1) * 256 + (l & 15) * 16;
    const unsigned xo = (g & 1) * 16384 + (g >> 1) * 256 + (l & 15) * 16;
    auto obs = [&](int s) { return TYPE == 3 ? wmap(WS_H + (s * 56 + idx) * 2048, p13) : TYPE == 1 ? wmap(WS_KF + (s * 128 + idx * 2) * 4096, p13) + 2048 : wmap(obase + ((s * 128 + idx * 2) << osh), p13); };
    const unsigned ov1 = ov + (TYPE == 1 ? 4096 : (1u << osh));
    auto ovk = [&](int k) { return ((k >> 1) ? ov1 : ov) + (k & 1) * 512; };
    const unsigned tab_o = l < 3 ? 0x9000000u + ((type * 64 + idx) * 2 + par) * 8192 : obs(seg(l - 1));
    const unsigned tab_x = xsrc(l + 3);
    const int tab_f = l < 4 ? (l == 0 ? 0 : seg(min(l, 2))) : seg(TYPE == 3 ? l - 2 : l - 1);
    auto ereg = [](int k, int i) { return TYPE == 1 ? (k & 1) * 4 + (i >> 1) * 8 + (i & 1) * 2 : (k & 1) * 8 + i * 2; };
    auto piece = [&](const v16f (&a)[2], int k, int i) {
        const int e = ereg(k, i);
        return TYPE == 3 ? pk2(swiglu(a[0][e], a[1][e]), swiglu(a[0][e + 1], a[1][e + 1])) : pk2(a[k >> 1][e], a[k >> 1][e + 1]); };
    v16f accA[2], accB[2];
    v4u xf[4];
    asm volatile("s_waitcnt vmcnt(16)\n\ts_barrier" ::: "memory");
    AST(2);
    xf[0] = lds128(xo); xf[1] = lds128(xo + 512); xf[2] = lds128(xo + 1024);
    auto body = [&](v16f (&acc)[2], v16f (&prev)[2], int it) {
        unsigned so = __builtin_amdgcn_readlane(tab_x, it); const unsigned oo = __builtin_amdgcn_readlane(tab_o, it), d = ((it + 3) & 3) * 32768 + w * 8192;
        const int fv = __builtin_amdgcn_readlane(tab_f, it);
        const unsigned xa = (it & 3) * 32768 + xo, xn = ((it + 1) & 3) * 32768 + xo;
        v4u pk; float t0, t1, u0, u1;
#pragma unroll
        for (int kk = 0; kk < 32; ++kk) {
            if (kk == 2) {
                asm volatile("s_waitcnt vmcnt(8)\n\ts_barrier" ::: "memory");
            }
            xf[(kk + 3) & 3] = lds128(kk < 29 ? xa + (kk + 3) * 512 : xn + (kk - 29) * 512);
#pragma unroll
            for (int c = 0; c < 2; ++c) {
                const v4u& wv = wf[kk >> 1][(kk & 1) * 2 + c];
                if (kk == 0) { if (TYPE == 1) mf_xw0(acc[c], wv, xf[kk & 3]); else mf_wx0(acc[c], wv, xf[kk & 3]); }
                else { if (TYPE == 1) mf_xw(acc[c], wv, xf[kk & 3]); else mf_wx(acc[c], wv, xf[kk & 3]); }
                if (kk == 3 && c == 0) asm volatile("s_mov_b64 exec, 1\n\tbuffer_store_dword %1, off, %2, %3 sc1\n\ts_mov_b64 exec, -1" : "+s"(so) : "v"(fv), "s"(srd), "s"(flag) : "memory");
                const int q = (kk - 4) * 2 + c;
                if (kk >= 4) {
                    if (TYPE == 3) {
                        const int j = q / 6, st = q % 6, e = ereg(j >> 2, j & 3);
                        if (st == 0 && (j == 4 || j == 8)) st16(pk, rW, ovk((j >> 2) - 1), oo);
                        if (j < 8) {
                            if (st == 0) asm volatile("v_mul_f32 %0, 0xbfb8aa3b, %2\n\tv_mul_f32 %1, 0xbfb8aa3b, %3\n\tv_exp_f32 %0, %0" : "=&v"(t0), "=&v"(t1) : "v"(prev[0][e]), "v"(prev[0][e + 1]));
                            if (st == 1) asm volatile("v_exp_f32 %2, %2\n\tv_mul_f32 %0, %3, %4\n\tv_mul_f32 %1, %5, %6" : "=&v"(u0), "=&v"(u1), "+v"(t1) : "v"(prev[0][e]), "v"(prev[1][e]), "v"(prev[0][e + 1]), "v"(prev[1][e + 1]));
                            if (st == 2) asm volatile("v_add_f32 %0, 1.0, %0\n\tv_add_f32 %1, 1.0, %1\n\tv_rcp_f32 %0, %0" : "+v"(t0), "+v"(t1));
                            if (st == 3) asm volatile("v_rcp_f32 %1, %1\n\tv_mul_f32 %0, %0, %2" : "+v"(u0), "+v"(t1) : "v"(t0));
                            if (st == 4) asm volatile("v_mul_f32 %1, %1, %3\n\tv_cvt_pk_bf16_f32 %0, %2, %1" : "=v"(pk[j & 3]), "+v"(u1) : "v"(u0), "v"(t1));

                        }
                    } else if (q < 24) {
                        const int k = q / 6, st = q % 6;
                        if (st < 2) {
                            const int e0 = ereg(k, 2 * st), e1 = ereg(k, 2 * st + 1);
                            asm volatile("v_cvt_pk_bf16_f32 %0, %2, %3\n\tv_cvt_pk_bf16_f32 %1, %4, %5" : "=&v"(pk[2 * st]), "=&v"(pk[2 * st + 1]) : "v"(prev[k >> 1][e0]), "v"(prev[k >> 1][e0 + 1]), "v"(prev[k >> 1][e1]), "v"(prev[k >> 1][e1 + 1]));
                        }
                        if (st == 4) st16(pk, rW, ovk(k), oo);
                    }
                }
                if (kk >= 16 && (kk & 1) && c == 0) {
                    const int pc = (kk - 16) >> 1;
                    switch (pc & 3) { case 0: blds<0>(l16, srd, so + (pc >> 2) * 4096, d + (pc >> 2) * 4096); break; case 1: blds_n<1024>(l16, srd, so + (pc >> 2) * 4096); break;
                                      case 2: blds_n<2048>(l16, srd, so + (pc >> 2) * 4096); break; default: blds_n<3072>(l16, srd, so + (pc >> 2) * 4096); }
                }
            }
        }
    };
    auto flush = [&](v16f (&acc)[2], int it) {
        asm volatile("s_nop 11" : "+v"(acc[0]), "+v"(acc[1]));
#pragma unroll
        for (int k = 0; k < NS; ++k) st16(v4u{piece(acc, k, 0), piece(acc, k, 1), piece(acc, k, 2), piece(acc, k, 3)}, rW, ovk(k), obs(seg(it)));
        asm volatile("s_waitcnt vmcnt(0)" ::: "memory");
        if (l == 0) __hip_atomic_store((GAS int*)(ws + flag), it + 1 == nit ? 64 : seg(it + 1), RLX_AGENT);
    };
    asm volatile("" : "=v"(accB[0]), "=v"(accB[1]));
#pragma unroll 1
    for (int it = 0; it < nit; it += 2) {
        body(accA, accB, it);
        if (it == 0) { AST(3); flush(accA, 0); AST(4); ACK(6); }
        body(accB, accA, it + 1);
        if (it == 0 || it == nit - 2) flush(accB, it + 1);
    }
    AST(5); ACK(7);
    return go;
}

constexpr unsigned CT_FLAG = 32768;
constexpr unsigned CT_O = 98304;
DEVI float x16_max(float a) { auto r = __builtin_amdgcn_permlane16_swap(__float_as_uint(a), __float_as_uint(a), false, false); return fmaxf(__uint_as_float(r[0]), __uint_as_float(r[1])); }
DEVI float x32_max(float a) { auto r = __builtin_amdgcn_permlane32_swap(__float_as_uint(a), __float_as_uint(a), false, false); return fmaxf(__uint_as_float(r[0]), __uint_as_float(r[1])); }
DEVI float x16_add(float a) { auto r = __builtin_amdgcn_permlane16_swap(__float_as_uint(a), __float_as_uint(a), false, false); return __uint_as_float(r[0]) + __uint_as_float(r[1]); }
DEVI float x32_add(float a) { auto r = __builtin_amdgcn_permlane32_swap(__float_as_uint(a), __float_as_uint(a), false, false); return __uint_as_float(r[0]) + __uint_as_float(r[1]); }
DEVI void mfv(v4f& c, const v4u& a, const v4u& b) { asm volatile("v_mfma_f32_16x16x32_bf16 %0, %1, %2, %0" : "+v"(c) : "v"(a), "v"(b)); }
DEVI void mfv0(v4f& c, const v4u& a, const v4u& b) { asm volatile("v_mfma_f32_16x16x32_bf16 %0, %1, %2, 0" : "=&v"(c) : "v"(a), "v"(b)); }
DEVI void mfv16(v4f& c, const v2u& a, const v2u& b) { asm volatile("v_mfma_f32_16x16x16_bf16 %0, %1, %2, %0" : "+v"(c) : "v"(a), "v"(b)); }
DEVI void mfa(v4f& c, const v4u& a, const v4u& b) { asm volatile("v_mfma_f32_16x16x32_bf16 %0, %1, %2, %0" : "+v"(c) : "a"(a), "v"(b)); }
DEVI void poll_ge(char* ws, unsigned off, int want, int& known, int step, unsigned& budget, int sleep_per) {
    if (known >= want) return;
    GAS int* p = (GAS int*)(ws + off);
    int v;
    for (;;) {
        v = __hip_atomic_load(p, RLX_AGENT);
        if (__all(v >= want)) break;
        if (++budget > POLL_CAP) break;
        int d = want - __builtin_amdgcn_readfirstlane(v);
        if (sleep_per && d > 2) { for (int k = 2; k < d; ++k) __builtin_amdgcn_s_sleep(40); } else __builtin_amdgcn_s_sleep(2);
    }
    known = __all(v >= want + 2 * step) ? want + 2 * step : (__all(v >= want + step) ? want + step : want);
    asm volatile("" ::: "memory");
}

#define THR 4
#define STR2(x) #x
#define STR(x) STR2(x)
#define THROTTLE asm volatile("s_waitcnt vmcnt(" STR(THR) ")")
DEVI void phaseB_run(const u16* Wo, const u16* wd, const u16* x, u16* out, char* ws, char* wide, unsigned p13, int l, int w, int grp, int ngrp, int ci) {
    const int n = l & 15, g = l >> 4, q4 = (l >> 2) & 3, p4 = l & 3;
    const unsigned l16 = l * 16;
    const __amdgpu_buffer_rsrc_t rW = __builtin_amdgcn_make_buffer_rsrc(wide, 0, 0x9000000, 0x00020000);
    const unsigned lw = w * 32768;
    constexpr unsigned L_OF = 131072;
    constexpr unsigned L_RED = L_OF + 16384;
    constexpr unsigned L_HF = L_RED + 4096;
    const v4u srd = {RFL((size_t)wide), RFL((size_t)wide >> 32) & 0xffffu, 0x9000000u, 0x20000u};
    const int kf0 = (ci * 7) >> 1, nkf = (((ci + 1) * 7) >> 1) - kf0;
    unsigned budget = 0;
    v4u wo[8][8];
    {
        asm volatile("s_waitcnt vmcnt(0) lgkmcnt(0)\n\ts_barrier" ::: "memory");
        const int row = l >> 4;
        const unsigned o0 = row * 1024 + w * 256 + ((n ^ (row << 1)) * 16), o1 = row * 1024 + w * 256 + ((n ^ ((row | 4) << 1)) * 16) + 4096;
        const unsigned ra = lw + (4 * g + q4) * 256 + (((4 * (g & 1) + q4)) * 32) + p4 * 8;
#pragma unroll
        for (int r = 0; r < 2; ++r) {
            const char* src = (const char*)Wo + (size_t)(256 * ci + 128 * r) * 1024;
#pragma unroll 2
            for (int k = 0; k < 16; ++k) { glds16(src + o0, lw + k * 2048); glds16(src + o1, lw + k * 2048 + 1024); src += 8192; THROTTLE; }
            asm volatile("s_waitcnt vmcnt(0)" ::: "memory");
#pragma unroll
            for (int ks = 0; ks < 4; ++ks) {
#pragma unroll
                for (int ct = 0; ct < 8; ++ct) { v2u a = trread((ra ^ (ct * 32)) + ks * 8192), b = trread((ra ^ (ct * 32)) + ks * 8192 + 4096); wo[4 * r + ks][ct] = v4u{a.x, a.y, b.x, b.y}; }
                asm volatile("" : "+a"(wo[4 * r + ks][0]), "+a"(wo[4 * r + ks][1]), "+a"(wo[4 * r + ks][2]), "+a"(wo[4 * r + ks][3]), "+a"(wo[4 * r + ks][4]), "+a"(wo[4 * r + ks][5]), "+a"(wo[4 * r + ks][6]), "+a"(wo[4 * r + ks][7]) :: "memory");
            }
        }
        const char* src = (const char*)wd + (size_t)(32 * kf0) * 1024;
#pragma unroll 1
        for (int k = 0; k < 4 * nkf; ++k) { glds16(src + o0, lw + k * 2048); glds16(src + o1, lw + k * 2048 + 1024); src += 8192; THROTTLE; }
        asm volatile("s_waitcnt vmcnt(0)" ::: "memory");
#pragma unroll 1
        for (int ks = 0; ks < nkf; ++ks) {
            v4u t[8];
#pragma unroll
            for (int ct = 0; ct < 8; ++ct) { v2u a = trread((ra ^ (ct * 32)) + ks * 8192), b = trread((ra ^ (ct * 32)) + ks * 8192 + 4096); t[ct] = v4u{a.x, a.y, b.x, b.y}; }
            asm volatile("" : "+v"(t[0]), "+v"(t[1]), "+v"(t[2]), "+v"(t[3]), "+v"(t[4]), "+v"(t[5]), "+v"(t[6]), "+v"(t[7]) :: "memory");
#pragma unroll
            for (int ct = 0; ct < 8; ++ct) sts128(lw + ks * 8192 + ct * 1024 + l16, t[ct]);
        }
    }
    const int h = 2 * ci + (w >> 1), qt = w & 1;
    const int jf = ((h / NX) * 2 + (l & 1)) * NX + (h % NX);
    const int uid = n < 6 ? (n >> 1) * 64 + 2 * h + (n & 1) : 192 + kf0 + min(w, nkf - 1);
    const unsigned upoll = CT_UNIT + uid * 128 + (n < 4 ? (g & 1) * 4 : 0);
    const unsigned cpoll = CT_FLAG + (jf * 4 + ((l & 2) ? 3 : 0)) * 128, opoll = CT_O + (grp * 16 + 8 * (w >> 1) + (l & 7)) * 128;
    const unsigned k1o = qt ? l16 + 1024 : 1024;
    int ku = 0, kc = 0, ko = 0;
    unsigned aw = lw + l16, ao = L_OF + l16;
    asm volatile("" : "+v"(aw), "+v"(ao));
    const float CS = 0.08838834764831845f * 1.4426950408889634f;
    auto hstage = [&](int s) {
        const unsigned so = wmap(WS_H + (s * 56 + kf0 + min(w, nkf - 1)) * 2048, p13);
        blds<0>(l16, srd, so, L_HF + w * 2048); blds_n<1024>(l16, srd, so);
    };
    auto pload = [&](int s, v4u (&pp)[8], v2u& xr) {
        const int pbuf = s & (4 * ngrp - 1);
#pragma unroll
        for (int k = 0; k < 8; ++k) pp[k] = __builtin_amdgcn_raw_buffer_load_b128(rW, l16 + (k & 1) * 2048, wmap(WS_PB + ((pbuf * 16 + ci) * 16 + 8 * (w >> 1) + (k & 6)) * 2048 + (w & 1) * 1024, p13), 16);
        xr = *(const GAS v2u*)(x + (32 * s + 16 * (w & 1) + n) * 512 + 32 * ci + 16 * (w >> 1) + 4 * g);
    };
    auto psum = [&](const v4u (&pp)[8], v4f& keep) {
        v4f r0 = {0, 0, 0, 0}, r1 = r0;
#pragma unroll
        for (int k = 0; k < 8; ++k) {
            r0 += v4f{__uint_as_float(pp[k].x << 16), __uint_as_float(pp[k].x & 0xffff0000u), __uint_as_float(pp[k].y << 16), __uint_as_float(pp[k].y & 0xffff0000u)};
            r1 += v4f{__uint_as_float(pp[k].z << 16), __uint_as_float(pp[k].z & 0xffff0000u), __uint_as_float(pp[k].w << 16), __uint_as_float(pp[k].w & 0xffff0000u)};
        }
        sts128(L_RED + w * 1024 + l16, __builtin_bit_cast(v4u, (w >> 1) ? r0 : r1));
        keep = (w >> 1) ? r1 : r0;
    };
    auto pout = [&](int s, const v4f& keep, const v2u& xr) {
        v4f r = keep + __builtin_bit_cast(v4f, lds128(L_RED + (w ^ 2) * 1024 + l16));
        r[0] += __uint_as_float(xr.x << 16); r[1] += __uint_as_float(xr.x & 0xffff0000u); r[2] += __uint_as_float(xr.y << 16); r[3] += __uint_as_float(xr.y & 0xffff0000u);
        const unsigned long long o = (unsigned long long)pk2(r[2], r[3]) << 32 | pk2(r[0], r[1]);
        __hip_atomic_store((GAS unsigned long long*)(out + (32 * s + 16 * (w & 1) + n) * 512 + 32 * ci + 16 * (w >> 1) + 4 * g), o, RLX_AGENT);
    };
#define BT(k)
    poll_ge(ws, upoll, grp + 1, ku, ngrp, budget, 0);
    hstage(grp);
    asm volatile("s_waitcnt vmcnt(0) lgkmcnt(0)\n\ts_barrier" ::: "memory");
#pragma unroll 1
    for (int s = grp; s < 64; s += ngrp) {
        const int pbuf = s & (4 * ngrp - 1);
        v4f acc[2][8];
        v4u pp[8]; v2u xr;
        {
            v4u qf[4], vf[4][2]; v4f s0, s1, sm;
            {
                v4u kf[4][2];
                const unsigned qo = wmap(WS_QF + (s * 128 + h * 4) * 2048, p13) + qt * 1024, ko0 = wmap(WS_KF + (s * 128 + h * 4) * 4096, p13), ko1 = ko0 + 16384;
                v4u hf[4][2], bq[4];
                hf[0][0] = lds128(ao + (L_HF - L_OF)); hf[0][1] = lds128(ao + (L_HF - L_OF) + 1024);
                bq[0] = lds128(aw); bq[1] = lds128(aw + 1024); bq[2] = lds128(aw + 2048);
#pragma unroll
                for (int i = 0; i < 32; ++i) {
                    const int k = i >> 3, ct = i & 7;
                    if (i == 24 && nkf < 4) break;
                    if (i < 4) qf[i] = __builtin_amdgcn_raw_buffer_load_b128(rW, l16 + (i & 1) * 2048, qo + (i >> 1) * 4096, 16);
                    else if (i < 12) kf[(i - 4) >> 1][i & 1] = __builtin_amdgcn_raw_buffer_load_b128(rW, (i & 1) ? k1o : l16, (i < 8 ? ko0 : ko1) + ((i >> 1) & 1) * 4096, 16);
                    else if (i < 20) vf[(i - 12) >> 1][i & 1] = __builtin_amdgcn_raw_buffer_load_b128(rW, l16 + 2048 + (i & 1) * 1024, (i < 16 ? ko0 : ko1) + ((i >> 1) & 1) * 4096, 16);
                    if (i < 29) bq[(i + 3) & 3] = lds128(aw + (i + 3) * 1024);
                    if (k < 3 && (ct == 4 || ct == 5)) hf[k + 1][ct - 4] = lds128(ao + (L_HF - L_OF) + (k + 1) * 2048 + (ct - 4) * 1024);
                    if (k == 0) { mfv0(acc[0][ct], bq[i & 3], hf[k][0]); mfv0(acc[1][ct], bq[i & 3], hf[k][1]); } else { mfv(acc[0][ct], bq[i & 3], hf[k][0]); mfv(acc[1][ct], bq[i & 3], hf[k][1]); }
                    __builtin_amdgcn_sched_barrier(0);
                }
                mfv0(s0, kf[0][0], qf[0]); mfv0(s1, kf[0][1], qf[0]);
#pragma unroll
                for (int k = 1; k < 4; ++k) { mfv(s0, kf[k][0], qf[k]); mfv(s1, kf[k][1], qf[k]); }
            }
            BT(1);
            if (s + ngrp < 64) poll_ge(ws, upoll, s + ngrp + 1, ku, ngrp, budget, 0);
            if (s >= ngrp) poll_ge(ws, opoll, s - ngrp + 1, ko, 0, budget, 0);
            BT(0);
            poll_ge(ws, cpoll, s + 1, kc, ngrp, budget, 1);
            BT(2);
            v4u km[4], vm[2];
#pragma unroll
            for (int k = 0; k < 4; ++k) km[k] = __builtin_amdgcn_raw_buffer_load_b128(rW, (g * 8 + 2 * (n >> 2) + (n & 1)) * 16 + k * 512, wmap(WS_KM + (s * 32 + h) * 2048, p13), 16);
#pragma unroll
            for (int k = 0; k < 2; ++k) vm[k] = __builtin_amdgcn_raw_buffer_load_b128(rW, l16 + k * 1024, wmap(WS_VM + (s * 32 + h) * 2048, p13), 16);
            mfv0(sm, km[0], qf[0]);
#pragma unroll
            for (int k = 1; k < 4; ++k) mfv(sm, km[k], qf[k]);
            asm volatile("s_nop 7" : "+v"(s0), "+v"(s1), "+v"(sm));
            const int qi = 16 * qt + n;
            float mx = fmaxf(sm[0], sm[1]);
#pragma unroll
            for (int i = 0; i < 4; ++i) {
                s0[i] = (4 * g + i <= qi) ? s0[i] : -__builtin_inff(); s1[i] = (16 + 4 * g + i <= qi) ? s1[i] : -__builtin_inff();
                mx = fmaxf(mx, fmaxf(s0[i], s1[i]));
            }
            mx = x32_max(x16_max(mx));
            const float mc = -mx * CS;
            float p0[4], p1[4], pm0 = __builtin_amdgcn_exp2f(fmaf(sm[0], CS, mc)), pm1 = __builtin_amdgcn_exp2f(fmaf(sm[1], CS, mc));
            float ls = pm0 + pm1;
#pragma unroll
            for (int i = 0; i < 4; ++i) { p0[i] = __builtin_amdgcn_exp2f(fmaf(s0[i], CS, mc)); p1[i] = __builtin_amdgcn_exp2f(fmaf(s1[i], CS, mc)); ls += p0[i] + p1[i]; }
            ls = x32_add(x16_add(ls));
            const float il = __builtin_amdgcn_rcpf(ls);
            v4u pf = {pk2(p0[0], p0[1]), pk2(p0[2], p0[3]), pk2(p1[0], p1[1]), pk2(p1[2], p1[3])};
            v2u pma = {pk2(pm0, pm1), 0u}, pmb = {0u, pk2(pm0, pm1)};
            asm volatile("s_nop 1" : "+v"(pf), "+v"(pma), "+v"(pmb));
            v4f o[4][2];
#pragma unroll
            for (int k = 0; k < 4; ++k) { mfv0(o[k][0], vf[k][0], pf); mfv0(o[k][1], vf[k][1], pf); }
#pragma unroll
            for (int k = 0; k < 4; ++k) { mfv16(o[k][0], k < 2 ? v2u{vm[0].x, vm[0].y} : v2u{vm[0].z, vm[0].w}, (k & 1) ? pmb : pma); mfv16(o[k][1], k < 2 ? v2u{vm[1].x, vm[1].y} : v2u{vm[1].z, vm[1].w}, (k & 1) ? pmb : pma); }
            asm volatile("s_nop 7" : "+v"(o[0][0]), "+v"(o[0][1]), "+v"(o[1][0]), "+v"(o[1][1]), "+v"(o[2][0]), "+v"(o[2][1]), "+v"(o[3][0]), "+v"(o[3][1]));
#pragma unroll
            for (int k = 0; k < 4; ++k) sts128(L_OF + (qt * 8 + (w >> 1) * 4 + k) * 1024 + l16, pack8(o[k][0] * il, o[k][1] * il));
        }
        asm volatile("s_waitcnt lgkmcnt(0)\n\ts_barrier" ::: "memory");
        BT(3);
        v4u of[8][2];
#pragma unroll
        for (int ks = 0; ks < 8; ++ks) { of[ks][0] = lds128(ao + ks * 1024); of[ks][1] = lds128(ao + 8192 + ks * 1024); }
        hstage(min(s + ngrp, 63));
        pload(max(s - ngrp, 0), pp, xr);
        __builtin_amdgcn_sched_barrier(0);
#pragma unroll
        for (int u = 0; u < 4; ++u) {
#pragma unroll
            for (int ks = 0; ks < 8; ++ks) {
                const v4u &o0 = of[ks][0], &o1 = of[ks][1];
                mfa(acc[0][2 * u], wo[ks][2 * u], o0); mfa(acc[0][2 * u + 1], wo[ks][2 * u + 1], o0);
                mfa(acc[1][2 * u], wo[ks][2 * u], o1); mfa(acc[1][2 * u + 1], wo[ks][2 * u + 1], o1);
                if (u > 0 && (ks == 2 || ks == 4)) {
                    const int tt = ks == 4;
                    asm volatile("" : "+v"(acc[tt][2 * u - 2]), "+v"(acc[tt][2 * u - 1]));
                    st16(pack8(acc[tt][2 * u - 2], acc[tt][2 * u - 1]), rW, l16 + tt * 1024, wmap(WS_PB + ((pbuf * 16 + 4 * w + u - 1) * 16 + ci) * 2048, p13));
                }
            }
        }
        asm volatile("" : "+v"(pp[0]), "+v"(pp[1]), "+v"(pp[2]), "+v"(pp[3]), "+v"(pp[4]), "+v"(pp[5]), "+v"(pp[6]), "+v"(pp[7]), "+v"(xr));
        asm volatile("s_nop 7" : "+v"(acc[0][6]), "+v"(acc[0][7]), "+v"(acc[1][6]), "+v"(acc[1][7]));
        st16(pack8(acc[0][6], acc[0][7]), rW, l16, wmap(WS_PB + ((pbuf * 16 + 4 * w + 3) * 16 + ci) * 2048, p13));
        st16(pack8(acc[1][6], acc[1][7]), rW, l16 + 1024, wmap(WS_PB + ((pbuf * 16 + 4 * w + 3) * 16 + ci) * 2048, p13));
        BT(4);
        v4f keep;
        psum(pp, keep);
        asm volatile("s_waitcnt vmcnt(0) lgkmcnt(0)\n\ts_barrier" ::: "memory");
        if (w == 0 && l == 0) __hip_atomic_store((GAS int*)(ws + CT_O + (grp * 16 + ci) * 128), s + 1, RLX_AGENT);
        if (s >= ngrp) pout(s - ngrp, keep, xr);
        BT(5);
    }
    {
        const int s = 64 - ngrp + grp;
        v4u pp[8]; v2u xr; v4f keep;
        poll_ge(ws, opoll, s + 1, ko, 0, budget, 0);
        BT(6);
        pload(s, pp, xr);
        asm volatile("s_waitcnt lgkmcnt(0)\n\ts_barrier" ::: "memory");
        psum(pp, keep);
        asm volatile("s_waitcnt lgkmcnt(0)\n\ts_barrier" ::: "memory");
        pout(s, keep, xr);
        BT(7);
    }
}

#define SCORE_ASM \
    "s_mov_b32 s40, s48\n" \
    "s_and_b32 s41, s49, 0xffff\n" \
    "s_mov_b32 s42, 0x10000000\n" \
    "s_mov_b32 s43, 0x20000\n" \
    "s_mov_b32 s46, 0x400000\n" \
    "s_mov_b32 s47, 0x20000\n" \
    "s_lshl_b32 s81, s62, 10\n" \
    "s_mov_b32 s44, s50\n" \
    "s_and_b32 s45, s51, 0xffff\n" \
    "s_add_u32 m0, s81, 24576\n" \
    "s_mov_b32 s76, 0\n" \
    "buffer_load_dwordx4 v16, s[44:47], s76 offen lds\n" \
    "s_add_u32 m0, s81, 28672\n" \
    "s_mov_b32 s76, 131072\n" \
    "buffer_load_dwordx4 v17, s[44:47], s76 offen lds\n" \
    "s_add_u32 m0, s81, 32768\n" \
    "s_mov_b32 s76, 262144\n" \
    "buffer_load_dwordx4 v16, s[44:47], s76 offen lds\n" \
    "s_add_u32 m0, s81, 36864\n" \
    "s_mov_b32 s76, 393216\n" \
    "buffer_load_dwordx4 v17, s[44:47], s76 offen lds\n" \
    "s_add_u32 m0, s81, 40960\n" \
    "s_mov_b32 s76, 524288\n" \
    "buffer_load_dwordx4 v16, s[44:47], s76 offen lds\n" \
    "s_add_u32 m0, s81, 45056\n" \
    "s_mov_b32 s76, 655360\n" \
    "buffer_load_dwordx4 v17, s[44:47], s76 offen lds\n" \
    "s_add_u32 m0, s81, 49152\n" \
    "s_mov_b32 s76, 786432\n" \
    "buffer_load_dwordx4 v16, s[44:47], s76 offen lds\n" \
    "s_add_u32 m0, s81, 53248\n" \
    "s_mov_b32 s76, 917504\n" \
    "buffer_load_dwordx4 v17, s[44:47], s76 offen lds\n" \
    "s_add_u32 m0, s81, 57344\n" \
    "s_mov_b32 s76, 1048576\n" \
    "buffer_load_dwordx4 v16, s[44:47], s76 offen lds\n" \
    "s_add_u32 m0, s81, 61440\n" \
    "s_mov_b32 s76, 1179648\n" \
    "buffer_load_dwordx4 v17, s[44:47], s76 offen lds\n" \
    "s_add_u32 m0, s81, 65536\n" \
    "s_mov_b32 s76, 1310720\n" \
    "buffer_load_dwordx4 v16, s[44:47], s76 offen lds\n" \
    "s_add_u32 m0, s81, 69632\n" \
    "s_mov_b32 s76, 1441792\n" \
    "buffer_load_dwordx4 v17, s[44:47], s76 offen lds\n" \
    "s_add_u32 m0, s81, 73728\n" \
    "s_mov_b32 s76, 1572864\n" \
    "buffer_load_dwordx4 v16, s[44:47], s76 offen lds\n" \
    "s_add_u32 m0, s81, 77824\n" \
    "s_mov_b32 s76, 1703936\n" \
    "buffer_load_dwordx4 v17, s[44:47], s76 offen lds\n" \
    "s_add_u32 m0, s81, 81920\n" \
    "s_mov_b32 s76, 1835008\n" \
    "buffer_load_dwordx4 v16, s[44:47], s76 offen lds\n" \
    "s_add_u32 m0, s81, 86016\n" \
    "s_mov_b32 s76, 1966080\n" \
    "buffer_load_dwordx4 v17, s[44:47], s76 offen lds\n" \
    "s_add_u32 m0, s81, 90112\n" \
    "s_mov_b32 s76, 2097152\n" \
    "buffer_load_dwordx4 v16, s[44:47], s76 offen lds\n" \
    "s_add_u32 m0, s81, 94208\n" \
    "s_mov_b32 s76, 2228224\n" \
    "buffer_load_dwordx4 v17, s[44:47], s76 offen lds\n" \
    "s_add_u32 m0, s81, 98304\n" \
    "s_mov_b32 s76, 2359296\n" \
    "buffer_load_dwordx4 v16, s[44:47], s76 offen lds\n" \
    "s_add_u32 m0, s81, 102400\n" \
    "s_mov_b32 s76, 2490368\n" \
    "buffer_load_dwordx4 v17, s[44:47], s76 offen lds\n" \
    "s_add_u32 m0, s81, 106496\n" \
    "s_mov_b32 s76, 2621440\n" \
    "buffer_load_dwordx4 v16, s[44:47], s76 offen lds\n" \
    "s_add_u32 m0, s81, 110592\n" \
    "s_mov_b32 s76, 2752512\n" \
    "buffer_load_dwordx4 v17, s[44:47], s76 offen lds\n" \
    "s_add_u32 m0, s81, 114688\n" \
    "s_mov_b32 s76, 2883584\n" \
    "buffer_load_dwordx4 v16, s[44:47], s76 offen lds\n" \
    "s_add_u32 m0, s81, 118784\n" \
    "s_mov_b32 s76, 3014656\n" \
    "buffer_load_dwordx4 v17, s[44:47], s76 offen lds\n" \
    "s_add_u32 m0, s81, 122880\n" \
    "s_mov_b32 s76, 3145728\n" \
    "buffer_load_dwordx4 v16, s[44:47], s76 offen lds\n" \
    "s_add_u32 m0, s81, 126976\n" \
    "s_mov_b32 s76, 3276800\n" \
    "buffer_load_dwordx4 v17, s[44:47], s76 offen lds\n" \
    "s_add_u32 m0, s81, 131072\n" \
    "s_mov_b32 s76, 3407872\n" \
    "buffer_load_dwordx4 v16, s[44:47], s76 offen lds\n" \
    "s_add_u32 m0, s81, 135168\n" \
    "s_mov_b32 s76, 3538944\n" \
    "buffer_load_dwordx4 v17, s[44:47], s76 offen lds\n" \
    "s_add_u32 m0, s81, 139264\n" \
    "s_mov_b32 s76, 3670016\n" \
    "buffer_load_dwordx4 v16, s[44:47], s76 offen lds\n" \
    "s_add_u32 m0, s81, 143360\n" \
    "s_mov_b32 s76, 3801088\n" \
    "buffer_load_dwordx4 v17, s[44:47], s76 offen lds\n" \
    "s_add_u32 m0, s81, 147456\n" \
    "s_mov_b32 s76, 3932160\n" \
    "buffer_load_dwordx4 v16, s[44:47], s76 offen lds\n" \
    "s_add_u32 m0, s81, 151552\n" \
    "s_mov_b32 s76, 4063232\n" \
    "buffer_load_dwordx4 v17, s[44:47], s76 offen lds\n" \
    "v_mov_b32 v128, v12\n" \
    "v_mov_b32 v129, v13\n" \
    "v_mov_b32 v130, v14\n" \
    "v_mov_b32 v131, v15\n" \
    "v_xor_b32 v104, 16, v4\n" \
    "v_xor_b32 v105, 32, v4\n" \
    "v_xor_b32 v106, 48, v4\n" \
    "v_add_u32 v107, s81, v6\n" \
    "s_waitcnt vmcnt(24)\n" \
    "s_barrier\n" \
    "v_add_u32 v24, 0, v20\n" \
    "v_xor_b32 v25, 32, v24\n" \
    "ds_read_b64_tr_b16 a[0:1], v24 offset:0\n" \
    "ds_read_b64_tr_b16 a[2:3], v24 offset:8192\n" \
    "ds_read_b64_tr_b16 a[4:5], v24 offset:2048\n" \
    "ds_read_b64_tr_b16 a[6:7], v24 offset:10240\n" \
    "ds_read_b64_tr_b16 a[8:9], v24 offset:16384\n" \
    "ds_read_b64_tr_b16 a[10:11], v24 offset:24576\n" \
    "ds_read_b64_tr_b16 a[12:13], v24 offset:18432\n" \
    "ds_read_b64_tr_b16 a[14:15], v24 offset:26624\n" \
    "ds_read_b64_tr_b16 a[64:65], v25 offset:0\n" \
    "ds_read_b64_tr_b16 a[66:67], v25 offset:8192\n" \
    "ds_read_b64_tr_b16 a[68:69], v25 offset:2048\n" \
    "ds_read_b64_tr_b16 a[70:71], v25 offset:10240\n" \
    "s_waitcnt lgkmcnt(4)\n" \
    "ds_read_b64_tr_b16 a[72:73], v25 offset:16384\n" \
    "ds_read_b64_tr_b16 a[74:75], v25 offset:24576\n" \
    "ds_read_b64_tr_b16 a[76:77], v25 offset:18432\n" \
    "ds_read_b64_tr_b16 a[78:79], v25 offset:26624\n" \
    "s_waitcnt lgkmcnt(0)\n" \
    "s_barrier\n" \
    "s_mov_b32 s44, s52\n" \
    "s_and_b32 s45, s53, 0xffff\n" \
    "s_add_u32 m0, s81, 24576\n" \
    "s_mov_b32 s76, 0\n" \
    "buffer_load_dwordx4 v16, s[44:47], s76 offen lds\n" \
    "s_add_u32 m0, s81, 28672\n" \
    "s_mov_b32 s76, 131072\n" \
    "buffer_load_dwordx4 v17, s[44:47], s76 offen lds\n" \
    "s_add_u32 m0, s81, 32768\n" \
    "s_mov_b32 s76, 262144\n" \
    "buffer_load_dwordx4 v16, s[44:47], s76 offen lds\n" \
    "s_add_u32 m0, s81, 36864\n" \
    "s_mov_b32 s76, 393216\n" \
    "buffer_load_dwordx4 v17, s[44:47], s76 offen lds\n" \
    "s_add_u32 m0, s81, 40960\n" \
    "s_mov_b32 s76, 524288\n" \
    "buffer_load_dwordx4 v16, s[44:47], s76 offen lds\n" \
    "s_add_u32 m0, s81, 45056\n" \
    "s_mov_b32 s76, 655360\n" \
    "buffer_load_dwordx4 v17, s[44:47], s76 offen lds\n" \
    "s_add_u32 m0, s81, 49152\n" \
    "s_mov_b32 s76, 786432\n" \
    "buffer_load_dwordx4 v16, s[44:47], s76 offen lds\n" \
    "s_add_u32 m0, s81, 53248\n" \
    "s_mov_b32 s76, 917504\n" \
    "buffer_load_dwordx4 v17, s[44:47], s76 offen lds\n" \
    "s_waitcnt vmcnt(24)\n" \
    "s_barrier\n" \
    "v_add_u32 v24, 32768, v20\n" \
    "v_xor_b32 v25, 32, v24\n" \
    "ds_read_b64_tr_b16 a[16:17], v24 offset:0\n" \
    "ds_read_b64_tr_b16 a[18:19], v24 offset:8192\n" \
    "ds_read_b64_tr_b16 a[20:21], v24 offset:2048\n" \
    "ds_read_b64_tr_b16 a[22:23], v24 offset:10240\n" \
    "ds_read_b64_tr_b16 a[24:25], v24 offset:16384\n" \
    "ds_read_b64_tr_b16 a[26:27], v24 offset:24576\n" \
    "ds_read_b64_tr_b16 a[28:29], v24 offset:18432\n" \
    "ds_read_b64_tr_b16 a[30:31], v24 offset:26624\n" \
    "ds_read_b64_tr_b16 a[80:81], v25 offset:0\n" \
    "ds_read_b64_tr_b16 a[82:83], v25 offset:8192\n" \
    "ds_read_b64_tr_b16 a[84:85], v25 offset:2048\n" \
    "ds_read_b64_tr_b16 a[86:87], v25 offset:10240\n" \
    "s_waitcnt lgkmcnt(4)\n" \
    "ds_read_b64_tr_b16 a[88:89], v25 offset:16384\n" \
    "ds_read_b64_tr_b16 a[90:91], v25 offset:24576\n" \
    "ds_read_b64_tr_b16 a[92:93], v25 offset:18432\n" \
    "ds_read_b64_tr_b16 a[94:95], v25 offset:26624\n" \
    "s_waitcnt lgkmcnt(0)\n" \
    "s_barrier\n" \
    "s_add_u32 m0, s81, 57344\n" \
    "s_mov_b32 s76, 1048576\n" \
    "buffer_load_dwordx4 v16, s[44:47], s76 offen lds\n" \
    "s_add_u32 m0, s81, 61440\n" \
    "s_mov_b32 s76, 1179648\n" \
    "buffer_load_dwordx4 v17, s[44:47], s76 offen lds\n" \
    "s_add_u32 m0, s81, 65536\n" \
    "s_mov_b32 s76, 1310720\n" \
    "buffer_load_dwordx4 v16, s[44:47], s76 offen lds\n" \
    "s_add_u32 m0, s81, 69632\n" \
    "s_mov_b32 s76, 1441792\n" \
    "buffer_load_dwordx4 v17, s[44:47], s76 offen lds\n" \
    "s_add_u32 m0, s81, 73728\n" \
    "s_mov_b32 s76, 1572864\n" \
    "buffer_load_dwordx4 v16, s[44:47], s76 offen lds\n" \
    "s_add_u32 m0, s81, 77824\n" \
    "s_mov_b32 s76, 1703936\n" \
    "buffer_load_dwordx4 v17, s[44:47], s76 offen lds\n" \
    "s_add_u32 m0, s81, 81920\n" \
    "s_mov_b32 s76, 1835008\n" \
    "buffer_load_dwordx4 v16, s[44:47], s76 offen lds\n" \
    "s_add_u32 m0, s81, 86016\n" \
    "s_mov_b32 s76, 1966080\n" \
    "buffer_load_dwordx4 v17, s[44:47], s76 offen lds\n" \
    "s_waitcnt vmcnt(24)\n" \
    "s_barrier\n" \
    "v_add_u32 v24, 65536, v20\n" \
    "v_xor_b32 v25, 32, v24\n" \
    "ds_read_b64_tr_b16 a[32:33], v24 offset:0\n" \
    "ds_read_b64_tr_b16 a[34:35], v24 offset:8192\n" \
    "ds_read_b64_tr_b16 a[36:37], v24 offset:2048\n" \
    "ds_read_b64_tr_b16 a[38:39], v24 offset:10240\n" \
    "ds_read_b64_tr_b16 a[40:41], v24 offset:16384\n" \
    "ds_read_b64_tr_b16 a[42:43], v24 offset:24576\n" \
    "ds_read_b64_tr_b16 a[44:45], v24 offset:18432\n" \
    "ds_read_b64_tr_b16 a[46:47], v24 offset:26624\n" \
    "ds_read_b64_tr_b16 a[96:97], v25 offset:0\n" \
    "ds_read_b64_tr_b16 a[98:99], v25 offset:8192\n" \
    "ds_read_b64_tr_b16 a[100:101], v25 offset:2048\n" \
    "ds_read_b64_tr_b16 a[102:103], v25 offset:10240\n" \
    "s_waitcnt lgkmcnt(4)\n" \
    "ds_read_b64_tr_b16 a[104:105], v25 offset:16384\n" \
    "ds_read_b64_tr_b16 a[106:107], v25 offset:24576\n" \
    "ds_read_b64_tr_b16 a[108:109], v25 offset:18432\n" \
    "ds_read_b64_tr_b16 a[110:111], v25 offset:26624\n" \
    "s_waitcnt lgkmcnt(0)\n" \
    "s_barrier\n" \
    "s_add_u32 m0, s81, 90112\n" \
    "s_mov_b32 s76, 2097152\n" \
    "buffer_load_dwordx4 v16, s[44:47], s76 offen lds\n" \
    "s_add_u32 m0, s81, 94208\n" \
    "s_mov_b32 s76, 2228224\n" \
    "buffer_load_dwordx4 v17, s[44:47], s76 offen lds\n" \
    "s_add_u32 m0, s81, 98304\n" \
    "s_mov_b32 s76, 2359296\n" \
    "buffer_load_dwordx4 v16, s[44:47], s76 offen lds\n" \
    "s_add_u32 m0, s81, 102400\n" \
    "s_mov_b32 s76, 2490368\n" \
    "buffer_load_dwordx4 v17, s[44:47], s76 offen lds\n" \
    "s_add_u32 m0, s81, 106496\n" \
    "s_mov_b32 s76, 2621440\n" \
    "buffer_load_dwordx4 v16, s[44:47], s76 offen lds\n" \
    "s_add_u32 m0, s81, 110592\n" \
    "s_mov_b32 s76, 2752512\n" \
    "buffer_load_dwordx4 v17, s[44:47], s76 offen lds\n" \
    "s_add_u32 m0, s81, 114688\n" \
    "s_mov_b32 s76, 2883584\n" \
    "buffer_load_dwordx4 v16, s[44:47], s76 offen lds\n" \
    "s_add_u32 m0, s81, 118784\n" \
    "s_mov_b32 s76, 3014656\n" \
    "buffer_load_dwordx4 v17, s[44:47], s76 offen lds\n" \
    "s_waitcnt vmcnt(24)\n" \
    "s_barrier\n" \
    "v_add_u32 v24, 98304, v20\n" \
    "v_xor_b32 v25, 32, v24\n" \
    "ds_read_b64_tr_b16 a[48:49], v24 offset:0\n" \
    "ds_read_b64_tr_b16 a[50:51], v24 offset:8192\n" \
    "ds_read_b64_tr_b16 a[52:53], v24 offset:2048\n" \
    "ds_read_b64_tr_b16 a[54:55], v24 offset:10240\n" \
    "ds_read_b64_tr_b16 a[56:57], v24 offset:16384\n" \
    "ds_read_b64_tr_b16 a[58:59], v24 offset:24576\n" \
    "ds_read_b64_tr_b16 a[60:61], v24 offset:18432\n" \
    "ds_read_b64_tr_b16 a[62:63], v24 offset:26624\n" \
    "ds_read_b64_tr_b16 a[112:113], v25 offset:0\n" \
    "ds_read_b64_tr_b16 a[114:115], v25 offset:8192\n" \
    "ds_read_b64_tr_b16 a[116:117], v25 offset:2048\n" \
    "ds_read_b64_tr_b16 a[118:119], v25 offset:10240\n" \
    "s_waitcnt lgkmcnt(4)\n" \
    "ds_read_b64_tr_b16 a[120:121], v25 offset:16384\n" \
    "ds_read_b64_tr_b16 a[122:123], v25 offset:24576\n" \
    "ds_read_b64_tr_b16 a[124:125], v25 offset:18432\n" \
    "ds_read_b64_tr_b16 a[126:127], v25 offset:26624\n" \
    "s_waitcnt lgkmcnt(0)\n" \
    "s_barrier\n" \
    "s_add_u32 m0, s81, 122880\n" \
    "s_mov_b32 s76, 3145728\n" \
    "buffer_load_dwordx4 v16, s[44:47], s76 offen lds\n" \
    "s_add_u32 m0, s81, 126976\n" \
    "s_mov_b32 s76, 3276800\n" \
    "buffer_load_dwordx4 v17, s[44:47], s76 offen lds\n" \
    "s_add_u32 m0, s81, 131072\n" \
    "s_mov_b32 s76, 3407872\n" \
    "buffer_load_dwordx4 v16, s[44:47], s76 offen lds\n" \
    "s_add_u32 m0, s81, 135168\n" \
    "s_mov_b32 s76, 3538944\n" \
    "buffer_load_dwordx4 v17, s[44:47], s76 offen lds\n" \
    "s_add_u32 m0, s81, 139264\n" \
    "s_mov_b32 s76, 3670016\n" \
    "buffer_load_dwordx4 v16, s[44:47], s76 offen lds\n" \
    "s_add_u32 m0, s81, 143360\n" \
    "s_mov_b32 s76, 3801088\n" \
    "buffer_load_dwordx4 v17, s[44:47], s76 offen lds\n" \
    "s_add_u32 m0, s81, 147456\n" \
    "s_mov_b32 s76, 3932160\n" \
    "buffer_load_dwordx4 v16, s[44:47], s76 offen lds\n" \
    "s_add_u32 m0, s81, 151552\n" \
    "s_mov_b32 s76, 4063232\n" \
    "buffer_load_dwordx4 v17, s[44:47], s76 offen lds\n" \
    "s_waitcnt vmcnt(24)\n" \
    "s_barrier\n" \
    "v_add_u32 v24, 0, v20\n" \
    "v_xor_b32 v25, 32, v24\n" \
    "ds_read_b64_tr_b16 a[128:129], v24 offset:0\n" \
    "ds_read_b64_tr_b16 a[130:131], v24 offset:8192\n" \
    "ds_read_b64_tr_b16 a[132:133], v24 offset:2048\n" \
    "ds_read_b64_tr_b16 a[134:135], v24 offset:10240\n" \
    "ds_read_b64_tr_b16 a[136:137], v24 offset:16384\n" \
    "ds_read_b64_tr_b16 a[138:139], v24 offset:24576\n" \
    "ds_read_b64_tr_b16 a[140:141], v24 offset:18432\n" \
    "ds_read_b64_tr_b16 a[142:143], v24 offset:26624\n" \
    "ds_read_b64_tr_b16 a[192:193], v25 offset:0\n" \
    "ds_read_b64_tr_b16 a[194:195], v25 offset:8192\n" \
    "ds_read_b64_tr_b16 a[196:197], v25 offset:2048\n" \
    "ds_read_b64_tr_b16 a[198:199], v25 offset:10240\n" \
    "s_waitcnt lgkmcnt(4)\n" \
    "ds_read_b64_tr_b16 a[200:201], v25 offset:16384\n" \
    "ds_read_b64_tr_b16 a[202:203], v25 offset:24576\n" \
    "ds_read_b64_tr_b16 a[204:205], v25 offset:18432\n" \
    "ds_read_b64_tr_b16 a[206:207], v25 offset:26624\n" \
    "s_waitcnt lgkmcnt(0)\n" \
    "s_barrier\n" \
    "s_mov_b32 s44, s54\n" \
    "s_and_b32 s45, s55, 0xffff\n" \
    "s_add_u32 m0, s81, 24576\n" \
    "s_mov_b32 s76, 0\n" \
    "buffer_load_dwordx4 v18, s[44:47], s76 offen lds\n" \
    "s_add_u32 m0, s81, 28672\n" \
    "s_mov_b32 s76, 8192\n" \
    "buffer_load_dwordx4 v18, s[44:47], s76 offen lds\n" \
    "s_add_u32 m0, s81, 32768\n" \
    "s_mov_b32 s76, 16384\n" \
    "buffer_load_dwordx4 v19, s[44:47], s76 offen lds\n" \
    "s_add_u32 m0, s81, 36864\n" \
    "s_mov_b32 s76, 24576\n" \
    "buffer_load_dwordx4 v19, s[44:47], s76 offen lds\n" \
    "s_add_u32 m0, s81, 40960\n" \
    "s_mov_b32 s76, 32768\n" \
    "buffer_load_dwordx4 v18, s[44:47], s76 offen lds\n" \
    "s_add_u32 m0, s81, 45056\n" \
    "s_mov_b32 s76, 40960\n" \
    "buffer_load_dwordx4 v18, s[44:47], s76 offen lds\n" \
    "s_add_u32 m0, s81, 49152\n" \
    "s_mov_b32 s76, 49152\n" \
    "buffer_load_dwordx4 v19, s[44:47], s76 offen lds\n" \
    "s_add_u32 m0, s81, 53248\n" \
    "s_mov_b32 s76, 57344\n" \
    "buffer_load_dwordx4 v19, s[44:47], s76 offen lds\n" \
    "s_waitcnt vmcnt(24)\n" \
    "s_barrier\n" \
    "v_add_u32 v24, 32768, v20\n" \
    "v_xor_b32 v25, 32, v24\n" \
    "ds_read_b64_tr_b16 a[144:145], v24 offset:0\n" \
    "ds_read_b64_tr_b16 a[146:147], v24 offset:8192\n" \
    "ds_read_b64_tr_b16 a[148:149], v24 offset:2048\n" \
    "ds_read_b64_tr_b16 a[150:151], v24 offset:10240\n" \
    "ds_read_b64_tr_b16 a[152:153], v24 offset:16384\n" \
    "ds_read_b64_tr_b16 a[154:155], v24 offset:24576\n" \
    "ds_read_b64_tr_b16 a[156:157], v24 offset:18432\n" \
    "ds_read_b64_tr_b16 a[158:159], v24 offset:26624\n" \
    "ds_read_b64_tr_b16 a[208:209], v25 offset:0\n" \
    "ds_read_b64_tr_b16 a[210:211], v25 offset:8192\n" \
    "ds_read_b64_tr_b16 a[212:213], v25 offset:2048\n" \
    "ds_read_b64_tr_b16 a[214:215], v25 offset:10240\n" \
    "s_waitcnt lgkmcnt(4)\n" \
    "ds_read_b64_tr_b16 a[216:217], v25 offset:16384\n" \
    "ds_read_b64_tr_b16 a[218:219], v25 offset:24576\n" \
    "ds_read_b64_tr_b16 a[220:221], v25 offset:18432\n" \
    "ds_read_b64_tr_b16 a[222:223], v25 offset:26624\n" \
    "s_waitcnt lgkmcnt(0)\n" \
    "s_barrier\n" \
    "s_add_u32 m0, s81, 57344\n" \
    "s_mov_b32 s76, 65536\n" \
    "buffer_load_dwordx4 v18, s[44:47], s76 offen lds\n" \
    "s_add_u32 m0, s81, 61440\n" \
    "s_mov_b32 s76, 73728\n" \
    "buffer_load_dwordx4 v18, s[44:47], s76 offen lds\n" \
    "s_add_u32 m0, s81, 65536\n" \
    "s_mov_b32 s76, 81920\n" \
    "buffer_load_dwordx4 v19, s[44:47], s76 offen lds\n" \
    "s_add_u32 m0, s81, 69632\n" \
    "s_mov_b32 s76, 90112\n" \
    "buffer_load_dwordx4 v19, s[44:47], s76 offen lds\n" \
    "s_add_u32 m0, s81, 73728\n" \
    "s_mov_b32 s76, 98304\n" \
    "buffer_load_dwordx4 v18, s[44:47], s76 offen lds\n" \
    "s_add_u32 m0, s81, 77824\n" \
    "s_mov_b32 s76, 106496\n" \
    "buffer_load_dwordx4 v18, s[44:47], s76 offen lds\n" \
    "s_add_u32 m0, s81, 81920\n" \
    "s_mov_b32 s76, 114688\n" \
    "buffer_load_dwordx4 v19, s[44:47], s76 offen lds\n" \
    "s_add_u32 m0, s81, 86016\n" \
    "s_mov_b32 s76, 122880\n" \
    "buffer_load_dwordx4 v19, s[44:47], s76 offen lds\n" \
    "s_waitcnt vmcnt(24)\n" \
    "s_barrier\n" \
    "v_add_u32 v24, 65536, v20\n" \
    "v_xor_b32 v25, 32, v24\n" \
    "ds_read_b64_tr_b16 a[160:161], v24 offset:0\n" \
    "ds_read_b64_tr_b16 a[162:163], v24 offset:8192\n" \
    "ds_read_b64_tr_b16 a[164:165], v24 offset:2048\n" \
    "ds_read_b64_tr_b16 a[166:167], v24 offset:10240\n" \
    "ds_read_b64_tr_b16 a[168:169], v24 offset:16384\n" \
    "ds_read_b64_tr_b16 a[170:171], v24 offset:24576\n" \
    "ds_read_b64_tr_b16 a[172:173], v24 offset:18432\n" \
    "ds_read_b64_tr_b16 a[174:175], v24 offset:26624\n" \
    "ds_read_b64_tr_b16 a[224:225], v25 offset:0\n" \
    "ds_read_b64_tr_b16 a[226:227], v25 offset:8192\n" \
    "ds_read_b64_tr_b16 a[228:229], v25 offset:2048\n" \
    "ds_read_b64_tr_b16 a[230:231], v25 offset:10240\n" \
    "s_waitcnt lgkmcnt(4)\n" \
    "ds_read_b64_tr_b16 a[232:233], v25 offset:16384\n" \
    "ds_read_b64_tr_b16 a[234:235], v25 offset:24576\n" \
    "ds_read_b64_tr_b16 a[236:237], v25 offset:18432\n" \
    "ds_read_b64_tr_b16 a[238:239], v25 offset:26624\n" \
    "s_waitcnt vmcnt(16)\n" \
    "s_barrier\n" \
    "v_add_u32 v24, 98304, v20\n" \
    "v_xor_b32 v25, 32, v24\n" \
    "ds_read_b64_tr_b16 a[176:177], v24 offset:0\n" \
    "ds_read_b64_tr_b16 a[178:179], v24 offset:8192\n" \
    "ds_read_b64_tr_b16 a[180:181], v24 offset:2048\n" \
    "ds_read_b64_tr_b16 a[182:183], v24 offset:10240\n" \
    "ds_read_b64_tr_b16 a[184:185], v24 offset:16384\n" \
    "ds_read_b64_tr_b16 a[186:187], v24 offset:24576\n" \
    "ds_read_b64_tr_b16 a[188:189], v24 offset:18432\n" \
    "ds_read_b64_tr_b16 a[190:191], v24 offset:26624\n" \
    "ds_read_b64_tr_b16 a[240:241], v25 offset:0\n" \
    "ds_read_b64_tr_b16 a[242:243], v25 offset:8192\n" \
    "ds_read_b64_tr_b16 a[244:245], v25 offset:2048\n" \
    "ds_read_b64_tr_b16 a[246:247], v25 offset:10240\n" \
    "s_waitcnt lgkmcnt(4)\n" \
    "ds_read_b64_tr_b16 a[248:249], v25 offset:16384\n" \
    "ds_read_b64_tr_b16 a[250:251], v25 offset:24576\n" \
    "ds_read_b64_tr_b16 a[252:253], v25 offset:18432\n" \
    "ds_read_b64_tr_b16 a[254:255], v25 offset:26624\n" \
    "s_waitcnt vmcnt(8)\n" \
    "s_barrier\n" \
    "v_add_u32 v24, 0, v21\n" \
    "v_xor_b32 v25, 32, v24\n" \
    "v_xor_b32 v26, 64, v24\n" \
    "v_xor_b32 v27, 0x60, v24\n" \
    "ds_read_b64_tr_b16 v[192:193], v24 offset:0\n" \
    "ds_read_b64_tr_b16 v[194:195], v24 offset:1024\n" \
    "ds_read_b64_tr_b16 v[196:197], v24 offset:16384\n" \
    "ds_read_b64_tr_b16 v[198:199], v24 offset:17408\n" \
    "ds_read_b64_tr_b16 v[208:209], v25 offset:0\n" \
    "ds_read_b64_tr_b16 v[210:211], v25 offset:1024\n" \
    "ds_read_b64_tr_b16 v[212:213], v25 offset:16384\n" \
    "ds_read_b64_tr_b16 v[214:215], v25 offset:17408\n" \
    "ds_read_b64_tr_b16 v[224:225], v26 offset:0\n" \
    "ds_read_b64_tr_b16 v[226:227], v26 offset:1024\n" \
    "ds_read_b64_tr_b16 v[228:229], v26 offset:16384\n" \
    "ds_read_b64_tr_b16 v[230:231], v26 offset:17408\n" \
    "s_waitcnt lgkmcnt(4)\n" \
    "ds_read_b64_tr_b16 v[240:241], v27 offset:0\n" \
    "ds_read_b64_tr_b16 v[242:243], v27 offset:1024\n" \
    "ds_read_b64_tr_b16 v[244:245], v27 offset:16384\n" \
    "ds_read_b64_tr_b16 v[246:247], v27 offset:17408\n" \
    "s_waitcnt vmcnt(0)\n" \
    "s_barrier\n" \
    "v_add_u32 v24, 32768, v21\n" \
    "v_xor_b32 v25, 32, v24\n" \
    "v_xor_b32 v26, 64, v24\n" \
    "v_xor_b32 v27, 0x60, v24\n" \
    "ds_read_b64_tr_b16 v[200:201], v24 offset:0\n" \
    "ds_read_b64_tr_b16 v[202:203], v24 offset:1024\n" \
    "ds_read_b64_tr_b16 v[204:205], v24 offset:16384\n" \
    "ds_read_b64_tr_b16 v[206:207], v24 offset:17408\n" \
    "ds_read_b64_tr_b16 v[216:217], v25 offset:0\n" \
    "ds_read_b64_tr_b16 v[218:219], v25 offset:1024\n" \
    "ds_read_b64_tr_b16 v[220:221], v25 offset:16384\n" \
    "ds_read_b64_tr_b16 v[222:223], v25 offset:17408\n" \
    "ds_read_b64_tr_b16 v[232:233], v26 offset:0\n" \
    "ds_read_b64_tr_b16 v[234:235], v26 offset:1024\n" \
    "ds_read_b64_tr_b16 v[236:237], v26 offset:16384\n" \
    "ds_read_b64_tr_b16 v[238:239], v26 offset:17408\n" \
    "s_waitcnt lgkmcnt(4)\n" \
    "ds_read_b64_tr_b16 v[248:249], v27 offset:0\n" \
    "ds_read_b64_tr_b16 v[250:251], v27 offset:1024\n" \
    "ds_read_b64_tr_b16 v[252:253], v27 offset:16384\n" \
    "ds_read_b64_tr_b16 v[254:255], v27 offset:17408\n" \
    "s_mov_b32 s77, 0\n" \
    "CSGO_%=:\n" \
    "buffer_load_dwordx2 v[96:97], v131, s[40:43], 0 offen sc1\n" \
    "s_add_u32 s77, s77, 1\n" \
    "s_waitcnt vmcnt(0)\n" \
    "v_readlane_b32 s26, v96, 0\n" \
    "v_readlane_b32 s27, v97, 0\n" \
    "v_readlane_b32 s28, v96, 1\n" \
    "v_readlane_b32 s29, v97, 1\n" \
    "s_cmp_ge_u32 s77, 0x20000\n" \
    "s_cbranch_scc1 CSGOK_%=\n" \
    "s_add_u32 s28, s28, 0x100\n" \
    "s_addc_u32 s29, s29, 0\n" \
    "s_xor_b32 s28, s26, s28\n" \
    "s_lshr_b32 s28, s28, 8\n" \
    "s_xor_b32 s27, s27, s29\n" \
    "s_or_b32 s28, s28, s27\n" \
    "s_cbranch_scc0 CSGOK_%=\n" \
    "s_sleep 3\n" \
    "s_branch CSGO_%=\n" \
    "CSGOK_%=:\n" \
    "s_bfe_u32 s28, s26, 0x30003\n" \
    "s_mov_b32 s29, s67\n" \
    "s_and_b32 s67, s26, 63\n" \
    "s_cmp_lg_u32 s28, s29\n" \
    "s_cbranch_scc1 CSREDO_%=\n" \
    "s_and_b32 s27, s26, 7\n" \
    "s_lshl_b32 s27, s27, 12\n" \
    "s_and_b32 s70, s27, 0x2000\n" \
    "v_xor_b32 v8, s27, v8\n" \
    "s_xor_b32 s58, s58, s27\n" \
    "s_add_u32 s58, s58, s56\n" \
    "s_xor_b32 s59, s59, s27\n" \
    "s_add_u32 s59, s59, s56\n" \
    "s_add_u32 s66, s66, s70\n" \
    "s_cmp_eq_u32 s62, 0\n" \
    "s_cselect_b32 s26, s70, 0\n" \
    "s_add_u32 s64, s64, s26\n" \
    "s_mov_b32 s84, 0xcccccccc\n" \
    "s_mov_b32 s85, 0xcccccccc\n" \
    "s_mov_b32 s86, 0x3e0293ee\n" \
    "s_mov_b32 s87, 0x3e0293ee\n" \
    "s_cmp_eq_u32 s63, 0\n" \
    "s_cselect_b32 s88, 0x33333333, 0\n" \
    "s_cselect_b32 s89, 0, 0x33333333\n" \
    "s_mov_b32 s72, 0\n" \
    "s_mov_b32 s80, 0\n" \
    "s_mov_b32 s79, 0\n" \
    "s_mov_b32 s77, 0\n" \
    "s_mov_b32 s78, 1\n" \
    "s_mov_b32 s73, s56\n" \
    "s_mov_b32 s47, s59\n" \
    "s_add_u32 s48, s59, 1024\n" \
    "s_mov_b32 s45, 2752624\n" \
    "v_mov_b32 v12, 0x3f803f80\n" \
    "v_mov_b32 v13, 0x3f803f80\n" \
    "v_mov_b32 v14, 0x3f803f80\n" \
    "v_mov_b32 v15, 0x3f803f80\n" \
    "v_mov_b64 v[164:165], 0\n" \
    "v_mov_b64 v[166:167], 0\n" \
    "v_mov_b64 v[108:109], 0\n" \
    "v_mov_b64 v[110:111], 0\n" \
    "v_mov_b64 v[170:171], 0\n" \
    "s_mov_b32 s75, 2\n" \
    "s_cmp_lg_u32 s62, 3\n" \
    "s_cbranch_scc1 CSKV0_%=\n" \
    "CSKP0_%=:\n" \
    "buffer_load_dword v96, v130, s[40:43], 0 offen sc1\n" \
    "s_add_u32 s77, s77, 1\n" \
    "s_cmp_ge_u32 s77, 0x20000\n" \
    "s_cbranch_scc1 CSKD0_%=\n" \
    "s_waitcnt vmcnt(0)\n" \
    "v_cmp_gt_i32 vcc, s75, v96\n" \
    "s_cbranch_vccz CSKD0_%=\n" \
    "s_sleep 2\n" \
    "s_branch CSKP0_%=\n" \
    "CSKD0_%=:\n" \
    "s_waitcnt vmcnt(0)\n" \
    "CSKV0_%=:\n" \
    "s_waitcnt lgkmcnt(0)\n" \
    "s_barrier\n" \
    "s_lshr_b32 s76, s60, 8\n" \
    "s_and_b32 s76, s76, 0x2000\n" \
    "s_xor_b32 s76, s76, s70\n" \
    "s_or_b32 s76, s76, s60\n" \
    "s_add_u32 s76, s76, s69\n" \
    "s_lshl_b32 s39, s81, 1\n" \
    "s_add_u32 m0, s39, 24576\n" \
    "s_nop 0\n" \
    "buffer_load_dwordx4 v0, s[40:43], s76 offen offset:0 sc1 lds\n" \
    "buffer_load_dwordx4 v0, s[40:43], s76 offen offset:1024 sc1 lds\n" \
    "buffer_load_dwordx4 v[80:83], v0, s[40:43], s76 offen offset:2048 sc1\n" \
    "buffer_load_dwordx4 v[84:87], v0, s[40:43], s76 offen offset:3072 sc1\n" \
    "s_add_u32 s60, s60, 0x100000\n" \
    "s_memrealtime s[26:27]\n" \
    "v_mov_b64 v[48:49], 0\n" \
    "v_mov_b64 v[50:51], 0\n" \
    "v_mov_b64 v[52:53], 0\n" \
    "v_mov_b64 v[54:55], 0\n" \
    "v_mov_b64 v[56:57], 0\n" \
    "v_mov_b64 v[58:59], 0\n" \
    "v_mov_b64 v[60:61], 0\n" \
    "v_mov_b64 v[62:63], 0\n" \
    "v_mov_b64 v[176:177], 0\n" \
    "v_mov_b64 v[178:179], 0\n" \
    "v_mov_b64 v[180:181], 0\n" \
    "v_mov_b64 v[182:183], 0\n" \
    "v_mov_b64 v[184:185], 0\n" \
    "v_mov_b64 v[186:187], 0\n" \
    "v_mov_b64 v[188:189], 0\n" \
    "v_mov_b64 v[190:191], 0\n" \
    "s_waitcnt vmcnt(0) lgkmcnt(0)\n" \
    "s_mov_b32 s44, s26\n" \
    "s_mov_b32 s74, s68\n" \
    "s_add_u32 s82, s64, 2048\n" \
    "s_cmp_eq_u32 s62, 0\n" \
    "s_cselect_b32 s82, s82, s68\n" \
    "s_barrier\n" \
    "s_memrealtime s[26:27]\n" \
    "s_waitcnt lgkmcnt(0)\n" \
    "s_min_u32 s26, s26, -2\n" \
    "v_mov_b32 v100, s26\n" \
    "CSLOAD_%=:\n" \
    "s_mov_b32 m0, s81\n" \
    "s_add_u32 s76, s57, 32768\n" \
    "buffer_load_dwordx4 v1, s[40:43], s57 offen sc1 lds\n" \
    "s_add_u32 m0, s81, 4096\n" \
    "s_nop 0\n" \
    "buffer_load_dwordx4 v1, s[40:43], s76 offen sc1 lds\n" \
    "ds_read_b128 v[132:135], v0 offset:24576\n" \
    "ds_read_b128 v[148:151], v0 offset:25600\n" \
    "ds_read_b128 v[136:139], v0 offset:26624\n" \
    "ds_read_b128 v[152:155], v0 offset:27648\n" \
    "ds_read_b128 v[140:143], v0 offset:28672\n" \
    "ds_read_b128 v[156:159], v0 offset:29696\n" \
    "ds_read_b128 v[144:147], v0 offset:30720\n" \
    "ds_read_b128 v[160:163], v0 offset:31744\n" \
    "s_mov_b64 exec, s[88:89]\n" \
    "buffer_store_dwordx4 v[120:123], v11, s[40:43], s74 offen sc1\n" \
    "s_mov_b64 exec, 1\n" \
    "buffer_store_dword v100, off, s[40:43], s82 sc1\n" \
    "s_mov_b64 exec, -1\n" \
    "s_mov_b32 s74, s68\n" \
    "s_mov_b32 s82, s68\n" \
    "s_cmp_eq_u32 s72, 63\n" \
    "s_cbranch_scc1 CSLAST_%=\n" \
    "s_waitcnt vmcnt(3) lgkmcnt(0)\n" \
    "s_barrier\n" \
    "ds_read_b128 v[16:19], v2 offset:0\n" \
    "ds_read_b128 v[20:23], v2 offset:1024\n" \
    "ds_read_b128 v[24:27], v2 offset:2048\n" \
    "ds_read_b128 v[28:31], v2 offset:3072\n" \
    "s_waitcnt lgkmcnt(3)\n" \
    "v_smfmac_f32_16x16x64_bf16 v[48:51], v[16:19], a[0:7], v3\n" \
    "s_waitcnt lgkmcnt(2)\n" \
    "v_smfmac_f32_16x16x64_bf16 v[52:55], v[16:19], a[64:71], v3\n" \
    "s_waitcnt vmcnt(2) lgkmcnt(1)\n" \
    "v_smfmac_f32_16x16x64_bf16 v[48:51], v[20:23], a[8:15], v3\n" \
    "s_barrier\n" \
    "v_smfmac_f32_16x16x64_bf16 v[52:55], v[20:23], a[72:79], v3\n" \
    "ds_read_b128 v[32:35], v2 offset:4096\n" \
    "v_smfmac_f32_16x16x64_bf16 v[48:51], v[24:27], a[16:23], v3\n" \
    "ds_read_b128 v[36:39], v2 offset:5120\n" \
    "v_smfmac_f32_16x16x64_bf16 v[52:55], v[24:27], a[80:87], v3\n" \
    "ds_read_b128 v[40:43], v2 offset:6144\n" \
    "s_waitcnt lgkmcnt(3)\n" \
    "v_smfmac_f32_16x16x64_bf16 v[48:51], v[28:31], a[24:31], v3\n" \
    "ds_read_b128 v[44:47], v2 offset:7168\n" \
    "v_smfmac_f32_16x16x64_bf16 v[52:55], v[28:31], a[88:95], v3\n" \
    "s_waitcnt lgkmcnt(3)\n" \
    "v_smfmac_f32_16x16x64_bf16 v[48:51], v[32:35], a[32:39], v3\n" \
    "s_waitcnt lgkmcnt(2)\n" \
    "v_smfmac_f32_16x16x64_bf16 v[48:51], v[36:39], a[40:47], v3\n" \
    "s_waitcnt lgkmcnt(1)\n" \
    "v_smfmac_f32_16x16x64_bf16 v[48:51], v[40:43], a[48:55], v3\n" \
    "s_waitcnt lgkmcnt(0)\n" \
    "v_smfmac_f32_16x16x64_bf16 v[48:51], v[44:47], a[56:63], v3\n" \
    "s_nop 1\n" \
    "v_smfmac_f32_16x16x64_bf16 v[52:55], v[32:35], a[96:103], v3\n" \
    "s_nop 1\n" \
    "v_smfmac_f32_16x16x64_bf16 v[52:55], v[36:39], a[104:111], v3\n" \
    "s_nop 0\n" \
    "v_smfmac_f32_16x16x64_bf16 v[52:55], v[40:43], a[112:119], v3\n" \
    "v_add_f32 v48, v48, v50\n" \
    "v_smfmac_f32_16x16x64_bf16 v[52:55], v[44:47], a[120:127], v3\n" \
    "v_add_f32 v49, v49, v51\n" \
    "v_smfmac_f32_16x16x64_bf16 v[56:59], v[16:19], a[128:135], v3\n" \
    "v_cvt_pk_bf16_f32 v50, v48, v49\n" \
    "v_smfmac_f32_16x16x64_bf16 v[56:59], v[20:23], a[136:143], v3\n" \
    "s_nop 1\n" \
    "v_smfmac_f32_16x16x64_bf16 v[56:59], v[24:27], a[144:151], v3\n" \
    "ds_write_b16 v105, v50 offset:0\n" \
    "v_add_f32 v52, v52, v54\n" \
    "v_smfmac_f32_16x16x64_bf16 v[56:59], v[28:31], a[152:159], v3\n" \
    "v_add_f32 v53, v53, v55\n" \
    "v_cvt_pk_bf16_f32 v54, v52, v53\n" \
    "v_smfmac_f32_16x16x64_bf16 v[56:59], v[32:35], a[160:167], v3\n" \
    "ds_write_b16_d16_hi v106, v50 offset:0\n" \
    "v_smfmac_f32_16x16x64_bf16 v[56:59], v[36:39], a[168:175], v3\n" \
    "ds_write_b16 v105, v54 offset:8\n" \
    "v_smfmac_f32_16x16x64_bf16 v[56:59], v[40:43], a[176:183], v3\n" \
    "ds_write_b16_d16_hi v106, v54 offset:8\n" \
    "v_smfmac_f32_16x16x64_bf16 v[56:59], v[44:47], a[184:191], v3\n" \
    "v_cmp_u_f32 vcc, v48, v49\n" \
    "v_smfmac_f32_16x16x64_bf16 v[60:63], v[16:19], a[192:199], v3\n" \
    "s_nop 1\n" \
    "v_smfmac_f32_16x16x64_bf16 v[60:63], v[20:23], a[200:207], v3\n" \
    "s_cbranch_vccnz CSMFAIL_%=\n" \
    "CSMGO_%=:\n" \
    "v_smfmac_f32_16x16x64_bf16 v[60:63], v[24:27], a[208:215], v3\n" \
    "s_waitcnt lgkmcnt(0)\n" \
    "s_barrier\n" \
    "v_smfmac_f32_16x16x64_bf16 v[60:63], v[28:31], a[216:223], v3\n" \
    "ds_read_b128 v[64:67], v5 offset:0\n" \
    "v_add_f32 v56, v56, v58\n" \
    "v_smfmac_f32_16x16x64_bf16 v[60:63], v[32:35], a[224:231], v3\n" \
    "ds_read_b128 v[68:71], v5 offset:1024\n" \
    "v_add_f32 v57, v57, v59\n" \
    "v_smfmac_f32_16x16x64_bf16 v[60:63], v[36:39], a[232:239], v3\n" \
    "ds_read_b128 v[72:75], v5 offset:2048\n" \
    "v_cvt_pk_bf16_f32 v58, v56, v57\n" \
    "v_smfmac_f32_16x16x64_bf16 v[60:63], v[40:43], a[240:247], v3\n" \
    "ds_read_b128 v[76:79], v5 offset:3072\n" \
    "v_smfmac_f32_16x16x64_bf16 v[60:63], v[44:47], a[248:255], v3\n" \
    "buffer_load_dword v168, v129, s[40:43], s47 offen sc1\n" \
    "s_waitcnt lgkmcnt(3)\n" \
    "v_mfma_f32_16x16x32_bf16 v[88:91], v[132:135], v[64:67], 0\n" \
    "ds_write_b16 v4, v58 offset:0\n" \
    "ds_write_b16_d16_hi v104, v58 offset:0\n" \
    "s_nop 2\n" \
    "v_add_f32 v60, v60, v62\n" \
    "v_add_f32 v61, v61, v63\n" \
    "v_cvt_pk_bf16_f32 v62, v60, v61\n" \
    "v_mfma_f32_16x16x32_bf16 v[92:95], v[148:151], v[64:67], 0\n" \
    "buffer_load_dword v169, v129, s[40:43], s48 offen sc1\n" \
    "ds_write_b16 v4, v62 offset:8\n" \
    "ds_write_b16_d16_hi v104, v62 offset:8\n" \
    "s_waitcnt lgkmcnt(4)\n" \
    "v_mfma_f32_16x16x32_bf16 v[88:91], v[136:139], v[68:71], v[88:91]\n" \
    "v_mfma_f32_16x16x32_bf16 v[92:95], v[152:155], v[68:71], v[92:95]\n" \
    "v_mfma_f32_16x16x32_bf16 v[88:91], v[140:143], v[72:75], v[88:91]\n" \
    "s_waitcnt lgkmcnt(0)\n" \
    "s_barrier\n" \
    "ds_read_b128 v[16:19], v6 offset:0\n" \
    "ds_read_b128 v[20:23], v6 offset:1024\n" \
    "v_mfma_f32_16x16x32_bf16 v[92:95], v[156:159], v[72:75], v[92:95]\n" \
    "ds_read_b128 v[24:27], v6 offset:2048\n" \
    "v_mfma_f32_16x16x32_bf16 v[88:91], v[144:147], v[76:79], v[88:91]\n" \
    "ds_read_b128 v[28:31], v6 offset:3072\n" \
    "v_mfma_f32_16x16x32_bf16 v[92:95], v[160:163], v[76:79], v[92:95]\n" \
    "s_waitcnt lgkmcnt(3)\n" \
    "v_mfma_f32_16x16x32_bf16 v[172:175], v[16:19], v[64:67], 0\n" \
    "s_waitcnt lgkmcnt(2)\n" \
    "s_nop 0\n" \
    "v_mfma_f32_16x16x32_bf16 v[172:175], v[20:23], v[68:71], v[172:175]\n" \
    "s_waitcnt lgkmcnt(1)\n" \
    "s_nop 0\n" \
    "v_mfma_f32_16x16x32_bf16 v[172:175], v[24:27], v[72:75], v[172:175]\n" \
    "s_waitcnt lgkmcnt(0)\n" \
    "v_cndmask_b32 v88, v88, v92, s[84:85]\n" \
    "v_mfma_f32_16x16x32_bf16 v[172:175], v[28:31], v[76:79], v[172:175]\n" \
    "v_cndmask_b32 v89, v89, v93, s[84:85]\n" \
    "v_cndmask_b32 v90, v90, v94, s[84:85]\n" \
    "v_cndmask_b32 v91, v91, v95, s[84:85]\n" \
    "v_max3_f32 v93, v88, v89, v90\n" \
    "v_max_f32 v93, v93, v91\n" \
    "s_nop 1\n" \
    "v_max_f32_dpp v94, v93, v93 quad_perm:[2,3,0,1] row_mask:0xf bank_mask:0xf\n" \
    "v_max3_f32 v92, v172, v173, v94\n" \
    "v_max3_f32 v93, v172, v173, v94\n" \
    "s_nop 1\n" \
    "v_permlane16_swap_b32 v92, v93\n" \
    "v_max_f32 v94, v92, v93\n" \
    "v_max_f32 v95, v92, v93\n" \
    "s_nop 1\n" \
    "v_permlane32_swap_b32 v94, v95\n" \
    "v_max_f32 v92, v94, v95\n" \
    "v_mul_f32 v92, 0xbe0293ee, v92\n" \
    "v_pk_fma_f32 v[88:89], v[88:89], s[86:87], v[92:93] op_sel_hi:[1,1,0]\n" \
    "v_pk_fma_f32 v[90:91], v[90:91], s[86:87], v[92:93] op_sel_hi:[1,1,0]\n" \
    "v_pk_fma_f32 v[172:173], v[172:173], s[86:87], v[92:93] op_sel_hi:[1,1,0]\n" \
    "v_exp_f32 v88, v88\n" \
    "v_exp_f32 v89, v89\n" \
    "v_exp_f32 v90, v90\n" \
    "v_exp_f32 v91, v91\n" \
    "v_cvt_pk_bf16_f32 v34, v88, v89\n" \
    "v_cvt_pk_bf16_f32 v35, v90, v91\n" \
    "v_exp_f32 v172, v172\n" \
    "v_exp_f32 v173, v173\n" \
    "v_mov_b32_dpp v32, v34 quad_perm:[2,3,0,1] row_mask:0xf bank_mask:0xf\n" \
    "v_mov_b32_dpp v33, v35 quad_perm:[2,3,0,1] row_mask:0xf bank_mask:0xf\n" \
    "v_cvt_pk_bf16_f32 v164, v172, v173\n" \
    "v_cvt_pk_bf16_f32 v109, v172, v173\n" \
    "CSVGO_%=:\n" \
    "v_mfma_f32_16x16x32_bf16 v[112:115], v[80:83], v[32:35], 0\n" \
    "s_waitcnt vmcnt(0)\n" \
    "v_max_u32 v92, v168, v169\n" \
    "v_mfma_f32_16x16x32_bf16 v[112:115], v[168:171], v[164:167], v[112:115]\n" \
    "s_nop 1\n" \
    "v_mfma_f32_16x16x32_bf16 v[116:119], v[84:87], v[32:35], 0\n" \
    "s_nop 1\n" \
    "v_mfma_f32_16x16x32_bf16 v[116:119], v[168:171], v[108:111], v[116:119]\n" \
    "s_nop 1\n" \
    "v_cvt_pk_bf16_f32 v124, v112, v113\n" \
    "v_cvt_pk_bf16_f32 v125, v114, v115\n" \
    "v_mfma_f32_16x16x32_bf16 v[120:123], v[32:35], v[12:15], 0\n" \
    "s_nop 2\n" \
    "v_cvt_pk_bf16_f32 v126, v116, v117\n" \
    "v_cvt_pk_bf16_f32 v127, v118, v119\n" \
    "ds_write_b128 v7, v[124:127]\n" \
    "v_mfma_f32_16x16x32_bf16 v[120:123], v[164:167], v[12:15], v[120:123]\n" \
    "v_cmp_eq_u32 vcc, -1, v92\n" \
    "v_mov_b32 v131, v131\n" \
    "v_mov_b32 v131, v131\n" \
    "s_cbranch_vccnz CSVFAIL_%=\n" \
    "CSVOK_%=:\n" \
    "v_mfma_f32_16x16x32_bf16 v[112:115], v[12:15], v[12:15], 0\n" \
    "s_waitcnt lgkmcnt(0)\n" \
    "s_barrier\n" \
    "ds_read_b128 v[96:99], v0 offset:16384\n" \
    "ds_read_b128 v[100:103], v0 offset:17408\n" \
    "v_mfma_f32_16x16x32_bf16 v[116:119], v[12:15], v[12:15], 0\n" \
    "v_mov_b32 v131, v131\n" \
    "v_rcp_f32 v36, v122\n" \
    "v_rcp_f32 v37, v123\n" \
    "s_waitcnt lgkmcnt(1)\n" \
    "v_smfmac_f32_16x16x64_bf16 v[176:179], v[96:99], v[192:199], v3\n" \
    "s_waitcnt lgkmcnt(0)\n" \
    "v_smfmac_f32_16x16x64_bf16 v[176:179], v[100:103], v[200:207], v3\n" \
    "s_nop 1\n" \
    "v_smfmac_f32_16x16x64_bf16 v[180:183], v[96:99], v[208:215], v3\n" \
    "s_nop 1\n" \
    "v_smfmac_f32_16x16x64_bf16 v[180:183], v[100:103], v[216:223], v3\n" \
    "s_nop 0\n" \
    "v_smfmac_f32_16x16x64_bf16 v[184:187], v[96:99], v[224:231], v3\n" \
    "v_add_f32 v176, v176, v178\n" \
    "v_smfmac_f32_16x16x64_bf16 v[184:187], v[100:103], v[232:239], v3\n" \
    "v_add_f32 v177, v177, v179\n" \
    "v_smfmac_f32_16x16x64_bf16 v[188:191], v[96:99], v[240:247], v3\n" \
    "v_mul_f32 v176, v176, v36\n" \
    "v_smfmac_f32_16x16x64_bf16 v[188:191], v[100:103], v[248:255], v3\n" \
    "v_add_f32 v180, v180, v182\n" \
    "v_add_f32 v181, v181, v183\n" \
    "v_mul_f32 v177, v177, v37\n" \
    "v_mul_f32 v180, v180, v36\n" \
    "v_mul_f32 v181, v181, v37\n" \
    "v_pk_add_f32 v[184:185], v[184:185], v[186:187]\n" \
    "v_cvt_pk_bf16_f32 v124, v176, v180\n" \
    "v_pk_mul_f32 v[184:185], v[184:185], v[36:37]\n" \
    "v_pk_add_f32 v[188:189], v[188:189], v[190:191]\n" \
    "v_cvt_pk_bf16_f32 v126, v177, v181\n" \
    "v_pk_mul_f32 v[188:189], v[188:189], v[36:37]\n" \
    "v_cvt_pk_bf16_f32 v125, v184, v188\n" \
    "v_cvt_pk_bf16_f32 v127, v185, v189\n" \
    "buffer_store_dwordx4 v[124:127], v8, s[40:43], s73 offen sc1\n" \
    "s_memrealtime s[26:27]\n" \
    "s_add_u32 s66, s66, 128\n" \
    "s_waitcnt lgkmcnt(0)\n" \
    "s_mov_b32 s46, s26\n" \
    "ds_read_b128 v[120:123], v107\n" \
    "v_mov_b64 v[48:49], 0\n" \
    "v_mov_b64 v[50:51], 0\n" \
    "v_mov_b64 v[52:53], 0\n" \
    "v_mov_b64 v[54:55], 0\n" \
    "v_mov_b64 v[56:57], 0\n" \
    "v_mov_b64 v[58:59], 0\n" \
    "v_mov_b64 v[60:61], 0\n" \
    "v_mov_b64 v[62:63], 0\n" \
    "v_mov_b64 v[176:177], 0\n" \
    "v_mov_b64 v[178:179], 0\n" \
    "v_mov_b64 v[180:181], 0\n" \
    "v_mov_b64 v[182:183], 0\n" \
    "v_mov_b64 v[184:185], 0\n" \
    "v_mov_b64 v[186:187], 0\n" \
    "v_mov_b64 v[188:189], 0\n" \
    "v_mov_b64 v[190:191], 0\n" \
    "v_mov_b32 v100, s72\n" \
    "s_add_u32 s72, s72, 1\n" \
    "v_xor_b32 v8, 0x84000, v8\n" \
    "s_and_b32 s75, s72, 3\n" \
    "s_lshl_b32 s75, s75, 16\n" \
    "s_add_u32 s57, s58, s75\n" \
    "s_lshl_b32 s75, s75, 1\n" \
    "s_add_u32 s47, s59, s75\n" \
    "s_add_u32 s48, s47, 1024\n" \
    "s_sub_u32 s49, s46, s44\n" \
    "s_min_u32 s49, s49, 0x3ff\n" \
    "s_cmp_eq_u32 s78, 0\n" \
    "s_cselect_b32 s49, s49, 0\n" \
    "s_not_b32 s49, s49\n" \
    "s_mov_b32 s78, 0\n" \
    "s_cmp_lg_u32 s62, 0\n" \
    "s_cbranch_scc1 CSHK_%=\n" \
    "v_mov_b32 v100, s49\n" \
    "buffer_load_dwordx2 v[96:97], off, s[40:43], s66 sc1\n" \
    "CSHK_%=:\n" \
    "s_sleep 1\n" \
    "s_lshr_b32 s76, s60, 8\n" \
    "s_and_b32 s76, s76, 0x2000\n" \
    "s_xor_b32 s76, s76, s70\n" \
    "s_or_b32 s76, s76, s60\n" \
    "s_add_u32 s76, s76, s69\n" \
    "s_lshl_b32 s39, s81, 1\n" \
    "s_add_u32 m0, s39, 24576\n" \
    "s_nop 0\n" \
    "buffer_load_dwordx4 v0, s[40:43], s76 offen offset:0 sc1 lds\n" \
    "buffer_load_dwordx4 v0, s[40:43], s76 offen offset:1024 sc1 lds\n" \
    "buffer_load_dwordx4 v[80:83], v0, s[40:43], s76 offen offset:2048 sc1\n" \
    "buffer_load_dwordx4 v[84:87], v0, s[40:43], s76 offen offset:3072 sc1\n" \
    "s_add_u32 s60, s60, 0x100000\n" \
    "s_lshr_b32 s74, s61, 8\n" \
    "s_and_b32 s74, s74, 0x2000\n" \
    "s_xor_b32 s74, s74, s70\n" \
    "s_or_b32 s74, s74, s61\n" \
    "s_add_u32 s74, s74, s69\n" \
    "s_add_u32 s61, s61, 0x20000\n" \
    "s_mov_b32 s82, s64\n" \
    "s_waitcnt vmcnt(0) lgkmcnt(0)\n" \
    "s_cmp_lg_u32 s62, 0\n" \
    "s_cbranch_scc1 CSJOIN_%=\n" \
    "s_add_u32 s79, s79, 1\n" \
    "v_cmp_eq_u32 vcc, -1, v97\n" \
    "s_cbranch_vccnz CSNOW_%=\n" \
    "v_readfirstlane_b32 s44, v96\n" \
    "v_readfirstlane_b32 s45, v97\n" \
    "s_nop 1\n" \
    "CSGOTW_%=:\n" \
    "s_lshr_b32 s49, s45, 16\n" \
    "s_add_u32 s49, s49, s46\n" \
    "s_add_u32 s49, s49, -6\n" \
    "s_sub_u32 s39, s49, s44\n" \
    "s_add_u32 s39, s39, 2048\n" \
    "s_cmp_lt_u32 s39, 0x4000\n" \
    "s_cselect_b32 s44, s44, s49\n" \
    "s_sub_u32 s44, s44, 76\n" \
    "CSGR1_%=:\n" \
    "s_add_u32 s44, s44, 76\n" \
    "s_sub_u32 s39, s44, s49\n" \
    "s_cmp_lt_i32 s39, 0\n" \
    "s_cbranch_scc1 CSGR1_%=\n" \
    "CSWAITB_%=:\n" \
    "s_sub_u32 s75, s44, 16\n" \
    "s_branch CSWC2_%=\n" \
    "CSWS2_%=:\n" \
    "s_sleep 2\n" \
    "CSWC2_%=:\n" \
    "s_memrealtime s[26:27]\n" \
    "s_waitcnt lgkmcnt(0)\n" \
    "s_sub_u32 s39, s26, s75\n" \
    "s_cmp_lt_i32 s39, 0\n" \
    "s_cbranch_scc1 CSWS2_%=\n" \
    "s_memrealtime s[26:27]\n" \
    "s_sub_u32 s75, s44, 3\n" \
    "s_nop 1\n" \
    "CSWL2_%=:\n" \
    "s_memrealtime s[28:29]\n" \
    "s_waitcnt lgkmcnt(1)\n" \
    "s_sub_u32 s39, s26, s75\n" \
    "s_cmp_ge_i32 s39, 0\n" \
    "s_cbranch_scc1 CSWD2_%=\n" \
    "s_memrealtime s[26:27]\n" \
    "s_waitcnt lgkmcnt(1)\n" \
    "s_sub_u32 s39, s28, s75\n" \
    "s_cmp_ge_i32 s39, 0\n" \
    "s_cbranch_scc1 CSWD2_%=\n" \
    "s_memrealtime s[28:29]\n" \
    "s_waitcnt lgkmcnt(1)\n" \
    "s_sub_u32 s39, s26, s75\n" \
    "s_cmp_ge_i32 s39, 0\n" \
    "s_cbranch_scc1 CSWD2_%=\n" \
    "s_memrealtime s[26:27]\n" \
    "s_waitcnt lgkmcnt(1)\n" \
    "s_sub_u32 s39, s28, s75\n" \
    "s_cmp_ge_i32 s39, 0\n" \
    "s_cbranch_scc1 CSWD2_%=\n" \
    "s_memrealtime s[28:29]\n" \
    "s_waitcnt lgkmcnt(1)\n" \
    "s_sub_u32 s39, s26, s75\n" \
    "s_cmp_ge_i32 s39, 0\n" \
    "s_cbranch_scc1 CSWD2_%=\n" \
    "s_memrealtime s[26:27]\n" \
    "s_waitcnt lgkmcnt(1)\n" \
    "s_sub_u32 s39, s28, s75\n" \
    "s_cmp_ge_i32 s39, 0\n" \
    "s_cbranch_scc0 CSWL2_%=\n" \
    "CSWD2_%=:\n" \
    "s_min_u32 s39, s39, 3\n" \
    "s_sub_u32 s39, 3, s39\n" \
    "CSWR2_%=:\n" \
    "s_sub_u32 s39, s39, 1\n" \
    "s_cbranch_scc0 CSWR2_%=\n" \
    "CSJOIN_%=:\n" \
    "s_barrier\n" \
    "s_branch CSLOAD_%=\n" \
    "CSNOW_%=:\n" \
    "CSGW3_%=:\n" \
    "s_sleep 1\n" \
    "buffer_load_dwordx2 v[96:97], off, s[40:43], s66 sc1\n" \
    "CSGC3_%=:\n" \
    "s_add_u32 s79, s79, 1\n" \
    "s_cmp_ge_u32 s79, 0x10000\n" \
    "s_cbranch_scc1 CSGD3_%=\n" \
    "s_waitcnt vmcnt(0)\n" \
    "v_cmp_eq_u32 vcc, -1, v97\n" \
    "s_cbranch_vccnz CSGW3_%=\n" \
    "v_readfirstlane_b32 s44, v96\n" \
    "v_readfirstlane_b32 s45, v97\n" \
    "CSGD3_%=:\n" \
    "s_nop 1\n" \
    "s_branch CSGOTW_%=\n" \
    "CSMFAIL_%=:\n" \
    "v_max3_u32 v112, v16, v17, v18\n" \
    "v_max3_u32 v112, v112, v19, v20\n" \
    "v_max3_u32 v112, v112, v21, v22\n" \
    "v_max3_u32 v112, v112, v23, v24\n" \
    "v_max3_u32 v112, v112, v25, v26\n" \
    "v_max3_u32 v112, v112, v27, v28\n" \
    "v_max3_u32 v112, v112, v29, v30\n" \
    "v_max3_u32 v112, v112, v31, v32\n" \
    "v_max3_u32 v112, v112, v33, v34\n" \
    "v_max3_u32 v112, v112, v35, v36\n" \
    "v_max3_u32 v112, v112, v37, v38\n" \
    "v_max3_u32 v112, v112, v39, v40\n" \
    "v_max3_u32 v112, v112, v41, v42\n" \
    "v_max3_u32 v112, v112, v43, v44\n" \
    "v_max3_u32 v112, v112, v45, v46\n" \
    "v_max_u32 v112, v112, v47\n" \
    "v_cmp_eq_u32 vcc, -1, v112\n" \
    "s_cbranch_vccz CSMGO_%=\n" \
    "s_add_u32 s80, s80, 0x10000\n" \
    "s_cmp_lt_u32 s80, 0x40000000\n" \
    "s_cbranch_scc0 CSMGO_%=\n" \
    "CSMRE_%=:\n" \
    "v_mov_b64 v[48:49], 0\n" \
    "v_mov_b64 v[50:51], 0\n" \
    "v_mov_b64 v[52:53], 0\n" \
    "v_mov_b64 v[54:55], 0\n" \
    "v_mov_b64 v[56:57], 0\n" \
    "v_mov_b64 v[58:59], 0\n" \
    "v_mov_b64 v[60:61], 0\n" \
    "v_mov_b64 v[62:63], 0\n" \
    "s_waitcnt vmcnt(0) lgkmcnt(0)\n" \
    "s_cmp_lg_u32 s62, 0\n" \
    "s_cbranch_scc1 CSJOIN_%=\n" \
    "s_memrealtime s[26:27]\n" \
    "s_waitcnt lgkmcnt(0)\n" \
    "s_add_u32 s44, s44, 76\n" \
    "s_sub_u32 s49, s26, 16\n" \
    "s_mov_b32 s78, 1\n" \
    "s_sub_u32 s39, s49, s44\n" \
    "s_add_u32 s39, s39, 2048\n" \
    "s_cmp_lt_u32 s39, 0x4000\n" \
    "s_cselect_b32 s44, s44, s49\n" \
    "s_sub_u32 s44, s44, 76\n" \
    "CSGR4_%=:\n" \
    "s_add_u32 s44, s44, 76\n" \
    "s_sub_u32 s39, s44, s49\n" \
    "s_cmp_lt_i32 s39, 0\n" \
    "s_cbranch_scc1 CSGR4_%=\n" \
    "s_branch CSWAITB_%=\n" \
    "CSVFAIL_%=:\n" \
    "s_mov_b32 s78, 1\n" \
    "s_add_u32 s79, s79, 1\n" \
    "s_cmp_ge_u32 s79, 0x8000\n" \
    "s_cbranch_scc1 CSVOK_%=\n" \
    "s_add_u32 s80, s80, 1\n" \
    "buffer_load_dword v168, v129, s[40:43], s47 offen sc1\n" \
    "buffer_load_dword v169, v129, s[40:43], s48 offen sc1\n" \
    "s_waitcnt vmcnt(0)\n" \
    "s_branch CSVGO_%=\n" \
    "CSLAST_%=:\n" \
    "s_waitcnt vmcnt(2) lgkmcnt(0)\n" \
    "s_barrier\n" \
    "ds_read_b128 v[16:19], v2 offset:0\n" \
    "ds_read_b128 v[20:23], v2 offset:1024\n" \
    "ds_read_b128 v[24:27], v2 offset:2048\n" \
    "ds_read_b128 v[28:31], v2 offset:3072\n" \
    "ds_read_b128 v[32:35], v2 offset:4096\n" \
    "ds_read_b128 v[36:39], v2 offset:5120\n" \
    "ds_read_b128 v[40:43], v2 offset:6144\n" \
    "ds_read_b128 v[44:47], v2 offset:7168\n" \
    "s_waitcnt lgkmcnt(7)\n" \
    "v_smfmac_f32_16x16x64_bf16 v[56:59], v[16:19], a[128:135], v3\n" \
    "v_smfmac_f32_16x16x64_bf16 v[60:63], v[16:19], a[192:199], v3\n" \
    "s_waitcnt lgkmcnt(6)\n" \
    "v_smfmac_f32_16x16x64_bf16 v[56:59], v[20:23], a[136:143], v3\n" \
    "v_smfmac_f32_16x16x64_bf16 v[60:63], v[20:23], a[200:207], v3\n" \
    "s_waitcnt lgkmcnt(5)\n" \
    "v_smfmac_f32_16x16x64_bf16 v[56:59], v[24:27], a[144:151], v3\n" \
    "v_smfmac_f32_16x16x64_bf16 v[60:63], v[24:27], a[208:215], v3\n" \
    "s_waitcnt lgkmcnt(4)\n" \
    "v_smfmac_f32_16x16x64_bf16 v[56:59], v[28:31], a[152:159], v3\n" \
    "v_smfmac_f32_16x16x64_bf16 v[60:63], v[28:31], a[216:223], v3\n" \
    "s_waitcnt lgkmcnt(3)\n" \
    "v_smfmac_f32_16x16x64_bf16 v[56:59], v[32:35], a[160:167], v3\n" \
    "v_smfmac_f32_16x16x64_bf16 v[60:63], v[32:35], a[224:231], v3\n" \
    "s_waitcnt lgkmcnt(2)\n" \
    "v_smfmac_f32_16x16x64_bf16 v[56:59], v[36:39], a[168:175], v3\n" \
    "v_smfmac_f32_16x16x64_bf16 v[60:63], v[36:39], a[232:239], v3\n" \
    "s_waitcnt lgkmcnt(1)\n" \
    "v_smfmac_f32_16x16x64_bf16 v[56:59], v[40:43], a[176:183], v3\n" \
    "v_smfmac_f32_16x16x64_bf16 v[60:63], v[40:43], a[240:247], v3\n" \
    "s_waitcnt lgkmcnt(0)\n" \
    "v_smfmac_f32_16x16x64_bf16 v[56:59], v[44:47], a[184:191], v3\n" \
    "v_smfmac_f32_16x16x64_bf16 v[60:63], v[44:47], a[248:255], v3\n" \
    "v_max3_u32 v112, v16, v17, v18\n" \
    "v_max3_u32 v112, v112, v19, v20\n" \
    "v_max3_u32 v112, v112, v21, v22\n" \
    "v_max3_u32 v112, v112, v23, v24\n" \
    "v_max3_u32 v112, v112, v25, v26\n" \
    "v_max3_u32 v112, v112, v27, v28\n" \
    "v_max3_u32 v112, v112, v29, v30\n" \
    "v_max3_u32 v112, v112, v31, v32\n" \
    "v_max3_u32 v112, v112, v33, v34\n" \
    "v_max3_u32 v112, v112, v35, v36\n" \
    "v_max3_u32 v112, v112, v37, v38\n" \
    "v_max3_u32 v112, v112, v39, v40\n" \
    "v_max3_u32 v112, v112, v41, v42\n" \
    "v_max3_u32 v112, v112, v43, v44\n" \
    "v_max3_u32 v112, v112, v45, v46\n" \
    "v_max_u32 v112, v112, v47\n" \
    "v_cmp_eq_u32 vcc, -1, v112\n" \
    "s_cbranch_vccz CSLGO_%=\n" \
    "s_add_u32 s80, s80, 0x10000\n" \
    "s_cmp_lt_u32 s80, 0x40000000\n" \
    "s_cbranch_scc1 CSMRE_%=\n" \
    "CSLGO_%=:\n" \
    "v_pk_add_f32 v[56:57], v[56:57], v[58:59]\n" \
    "v_pk_add_f32 v[60:61], v[60:61], v[62:63]\n" \
    "v_cvt_pk_bf16_f32 v58, v56, v57\n" \
    "v_cvt_pk_bf16_f32 v62, v60, v61\n" \
    "ds_write_b16 v4, v58 offset:0\n" \
    "ds_write_b16_d16_hi v104, v58 offset:0\n" \
    "ds_write_b16 v4, v62 offset:8\n" \
    "ds_write_b16_d16_hi v104, v62 offset:8\n" \
    "ds_read_b128 v[120:123], v107\n" \
    "v_mov_b32 v100, 64\n" \
    "s_lshr_b32 s76, s61, 8\n" \
    "s_and_b32 s76, s76, 0x2000\n" \
    "s_xor_b32 s76, s76, s70\n" \
    "s_or_b32 s76, s76, s61\n" \
    "s_add_u32 s76, s76, s69\n" \
    "s_waitcnt lgkmcnt(0)\n" \
    "s_mov_b64 exec, s[88:89]\n" \
    "buffer_store_dwordx4 v[120:123], v11, s[40:43], s76 offen sc1\n" \
    "s_mov_b64 exec, -1\n" \
    "s_waitcnt vmcnt(0)\n" \
    "s_barrier\n" \
    "s_mov_b64 exec, 1\n" \
    "buffer_store_dword v100, off, s[40:43], s64 sc1\n" \
    "s_mov_b64 exec, -1\n" \
    "s_waitcnt vmcnt(0)\n"
#define VALUE_ASM \
    "s_mov_b32 s40, s48\n" \
    "s_and_b32 s41, s49, 0xffff\n" \
    "s_mov_b32 s42, 0x10000000\n" \
    "s_mov_b32 s43, 0x20000\n" \
    "s_mov_b32 s46, 0x400000\n" \
    "s_mov_b32 s47, 0x20000\n" \
    "s_mov_b32 s44, s50\n" \
    "s_and_b32 s45, s51, 0xffff\n" \
    "s_lshl_b32 s81, s59, 10\n" \
    "s_add_u32 m0, s81, 24576\n" \
    "s_mov_b32 s76, 0\n" \
    "buffer_load_dwordx4 v10, s[44:47], s76 offen lds\n" \
    "s_add_u32 m0, s81, 28672\n" \
    "s_mov_b32 s76, 262144\n" \
    "buffer_load_dwordx4 v10, s[44:47], s76 offen lds\n" \
    "s_add_u32 m0, s81, 32768\n" \
    "s_mov_b32 s76, 524288\n" \
    "buffer_load_dwordx4 v10, s[44:47], s76 offen lds\n" \
    "s_add_u32 m0, s81, 36864\n" \
    "s_mov_b32 s76, 786432\n" \
    "buffer_load_dwordx4 v10, s[44:47], s76 offen lds\n" \
    "s_add_u32 m0, s81, 40960\n" \
    "s_mov_b32 s76, 1048576\n" \
    "buffer_load_dwordx4 v10, s[44:47], s76 offen lds\n" \
    "s_add_u32 m0, s81, 45056\n" \
    "s_mov_b32 s76, 1310720\n" \
    "buffer_load_dwordx4 v10, s[44:47], s76 offen lds\n" \
    "s_add_u32 m0, s81, 49152\n" \
    "s_mov_b32 s76, 1572864\n" \
    "buffer_load_dwordx4 v10, s[44:47], s76 offen lds\n" \
    "s_add_u32 m0, s81, 53248\n" \
    "s_mov_b32 s76, 1835008\n" \
    "buffer_load_dwordx4 v10, s[44:47], s76 offen lds\n" \
    "s_add_u32 m0, s81, 57344\n" \
    "s_mov_b32 s76, 2097152\n" \
    "buffer_load_dwordx4 v10, s[44:47], s76 offen lds\n" \
    "s_add_u32 m0, s81, 61440\n" \
    "s_mov_b32 s76, 2359296\n" \
    "buffer_load_dwordx4 v10, s[44:47], s76 offen lds\n" \
    "s_add_u32 m0, s81, 65536\n" \
    "s_mov_b32 s76, 2621440\n" \
    "buffer_load_dwordx4 v10, s[44:47], s76 offen lds\n" \
    "s_add_u32 m0, s81, 69632\n" \
    "s_mov_b32 s76, 2883584\n" \
    "buffer_load_dwordx4 v10, s[44:47], s76 offen lds\n" \
    "s_add_u32 m0, s81, 73728\n" \
    "s_mov_b32 s76, 3145728\n" \
    "buffer_load_dwordx4 v10, s[44:47], s76 offen lds\n" \
    "s_add_u32 m0, s81, 77824\n" \
    "s_mov_b32 s76, 3407872\n" \
    "buffer_load_dwordx4 v10, s[44:47], s76 offen lds\n" \
    "s_add_u32 m0, s81, 81920\n" \
    "s_mov_b32 s76, 3670016\n" \
    "buffer_load_dwordx4 v10, s[44:47], s76 offen lds\n" \
    "s_add_u32 m0, s81, 86016\n" \
    "s_mov_b32 s76, 3932160\n" \
    "buffer_load_dwordx4 v10, s[44:47], s76 offen lds\n" \
    "s_mov_b32 s90, 0x3030303\n" \
    "s_mov_b32 s91, 0x3030303\n" \
    "v_cndmask_b32 v56, 0, v9, s[90:91]\n" \
    "s_mov_b32 s90, 0xc0c0c0c\n" \
    "s_mov_b32 s91, 0xc0c0c0c\n" \
    "v_cndmask_b32 v57, 0, v9, s[90:91]\n" \
    "s_mov_b32 s90, 0x30303030\n" \
    "s_mov_b32 s91, 0x30303030\n" \
    "v_cndmask_b32 v58, 0, v9, s[90:91]\n" \
    "s_mov_b32 s90, 0xc0c0c0c0\n" \
    "s_mov_b32 s91, 0xc0c0c0c0\n" \
    "v_cndmask_b32 v59, 0, v9, s[90:91]\n" \
    "v_mov_b32 v84, -1\n" \
    "v_mov_b32 v85, -1\n" \
    "v_mov_b32 v86, -1\n" \
    "v_mov_b32 v87, -1\n" \
    "s_waitcnt vmcnt(0)\n" \
    "s_barrier\n" \
    "ds_read_b64_tr_b16 v[128:129], v11 offset:0\n" \
    "ds_read_b64_tr_b16 v[130:131], v11 offset:4096\n" \
    "ds_read_b64_tr_b16 v[132:133], v11 offset:1024\n" \
    "ds_read_b64_tr_b16 v[134:135], v11 offset:5120\n" \
    "ds_read_b64_tr_b16 v[136:137], v11 offset:8192\n" \
    "ds_read_b64_tr_b16 v[138:139], v11 offset:12288\n" \
    "ds_read_b64_tr_b16 v[140:141], v11 offset:9216\n" \
    "ds_read_b64_tr_b16 v[142:143], v11 offset:13312\n" \
    "s_waitcnt lgkmcnt(4)\n" \
    "ds_read_b64_tr_b16 v[144:145], v11 offset:16384\n" \
    "ds_read_b64_tr_b16 v[146:147], v11 offset:20480\n" \
    "ds_read_b64_tr_b16 v[148:149], v11 offset:17408\n" \
    "ds_read_b64_tr_b16 v[150:151], v11 offset:21504\n" \
    "ds_read_b64_tr_b16 v[152:153], v11 offset:24576\n" \
    "ds_read_b64_tr_b16 v[154:155], v11 offset:28672\n" \
    "ds_read_b64_tr_b16 v[156:157], v11 offset:25600\n" \
    "ds_read_b64_tr_b16 v[158:159], v11 offset:29696\n" \
    "s_waitcnt lgkmcnt(4)\n" \
    "ds_read_b64_tr_b16 v[160:161], v11 offset:32768\n" \
    "ds_read_b64_tr_b16 v[162:163], v11 offset:36864\n" \
    "ds_read_b64_tr_b16 v[164:165], v11 offset:33792\n" \
    "ds_read_b64_tr_b16 v[166:167], v11 offset:37888\n" \
    "ds_read_b64_tr_b16 v[168:169], v11 offset:40960\n" \
    "ds_read_b64_tr_b16 v[170:171], v11 offset:45056\n" \
    "ds_read_b64_tr_b16 v[172:173], v11 offset:41984\n" \
    "ds_read_b64_tr_b16 v[174:175], v11 offset:46080\n" \
    "s_waitcnt lgkmcnt(4)\n" \
    "ds_read_b64_tr_b16 v[176:177], v11 offset:49152\n" \
    "ds_read_b64_tr_b16 v[178:179], v11 offset:53248\n" \
    "ds_read_b64_tr_b16 v[180:181], v11 offset:50176\n" \
    "ds_read_b64_tr_b16 v[182:183], v11 offset:54272\n" \
    "ds_read_b64_tr_b16 v[184:185], v11 offset:57344\n" \
    "ds_read_b64_tr_b16 v[186:187], v11 offset:61440\n" \
    "ds_read_b64_tr_b16 v[188:189], v11 offset:58368\n" \
    "ds_read_b64_tr_b16 v[190:191], v11 offset:62464\n" \
    "s_waitcnt lgkmcnt(4)\n" \
    "s_mov_b32 s72, 0\n" \
    "s_mov_b32 s80, 0\n" \
    "s_mov_b32 s79, 0\n" \
    "s_mov_b32 s84, 0\n" \
    "s_mov_b32 s78, 0\n" \
    "s_mov_b32 s73, s52\n" \
    "s_mov_b32 s83, s56\n" \
    "s_mov_b32 s46, 2752624\n" \
    "s_mov_b32 s77, 840\n" \
    "s_mov_b32 s82, 296\n" \
    "v_mov_b32 v88, 0\n" \
    "v_mov_b32 v89, s46\n" \
    "ds_write_b32 v88, v89 offset:20480\n" \
    "s_mov_b32 s75, 2\n" \
    "s_cmp_lg_u32 s59, 0\n" \
    "s_cbranch_scc1 CVKV0_%=\n" \
    "CVKP0_%=:\n" \
    "buffer_load_dword v96, v12, s[40:43], 0 offen sc1\n" \
    "s_add_u32 s79, s79, 1\n" \
    "s_cmp_ge_u32 s79, 0x20000\n" \
    "s_cbranch_scc1 CVKD0_%=\n" \
    "s_waitcnt vmcnt(0)\n" \
    "v_cmp_gt_i32 vcc, s75, v96\n" \
    "s_cbranch_vccz CVKD0_%=\n" \
    "s_sleep 2\n" \
    "s_branch CVKP0_%=\n" \
    "CVKD0_%=:\n" \
    "s_waitcnt vmcnt(0)\n" \
    "CVKV0_%=:\n" \
    "s_barrier\n" \
    "s_waitcnt lgkmcnt(0)\n" \
    "s_memrealtime s[26:27]\n" \
    "v_mov_b64 v[48:49], 0\n" \
    "v_mov_b64 v[50:51], 0\n" \
    "s_waitcnt lgkmcnt(0)\n" \
    "s_mov_b32 s45, s26\n" \
    "s_mov_b32 s86, 0\n" \
    "s_mov_b32 s88, 1\n" \
    "CVLOAD_%=:\n" \
    "s_mov_b32 m0, s81\n" \
    "s_add_u32 s76, s53, 32768\n" \
    "buffer_load_dwordx4 v1, s[40:43], s53 offen sc1 lds\n" \
    "s_add_u32 m0, s81, 4096\n" \
    "s_nop 0\n" \
    "buffer_load_dwordx4 v1, s[40:43], s76 offen sc1 lds\n" \
    "s_lshr_b32 s76, s58, 8\n" \
    "s_and_b32 s76, s76, 0x2000\n" \
    "s_xor_b32 s76, s76, s70\n" \
    "s_or_b32 s76, s76, s58\n" \
    "s_add_u32 s76, s76, s69\n" \
    "s_nop 7\n" \
    "s_cmp_eq_u32 s59, 0\n" \
    "s_cbranch_scc1 CVX0_%=\n" \
    "s_cmp_eq_u32 s59, 2\n" \
    "s_cbranch_scc1 CVX2_%=\n" \
    "buffer_load_dword v101, v12, s[40:43], 0 offen sc1\n" \
    "CVXD_%=:\n" \
    "s_waitcnt vmcnt(2) lgkmcnt(0)\n" \
    "s_barrier\n" \
    "ds_read_b128 v[16:19], v2 offset:0\n" \
    "ds_read_b128 v[20:23], v2 offset:1024\n" \
    "ds_read_b128 v[24:27], v2 offset:2048\n" \
    "ds_read_b128 v[28:31], v2 offset:3072\n" \
    "s_waitcnt vmcnt(1)\n" \
    "s_barrier\n" \
    "ds_read_b128 v[32:35], v2 offset:4096\n" \
    "s_waitcnt lgkmcnt(4)\n" \
    "v_smfmac_f32_16x16x64_bf16 v[48:51], v[16:19], v[128:135], v3\n" \
    "ds_read_b128 v[36:39], v2 offset:5120\n" \
    "s_waitcnt lgkmcnt(4)\n" \
    "v_smfmac_f32_16x16x64_bf16 v[48:51], v[20:23], v[136:143], v3\n" \
    "ds_read_b128 v[40:43], v2 offset:6144\n" \
    "s_waitcnt lgkmcnt(4)\n" \
    "v_smfmac_f32_16x16x64_bf16 v[48:51], v[24:27], v[144:151], v3\n" \
    "ds_read_b128 v[44:47], v2 offset:7168\n" \
    "s_waitcnt lgkmcnt(4)\n" \
    "v_smfmac_f32_16x16x64_bf16 v[48:51], v[28:31], v[152:159], v3\n" \
    "s_waitcnt lgkmcnt(3)\n" \
    "v_smfmac_f32_16x16x64_bf16 v[48:51], v[32:35], v[160:167], v3\n" \
    "s_waitcnt lgkmcnt(2)\n" \
    "v_smfmac_f32_16x16x64_bf16 v[48:51], v[36:39], v[168:175], v3\n" \
    "s_waitcnt lgkmcnt(1)\n" \
    "v_smfmac_f32_16x16x64_bf16 v[48:51], v[40:43], v[176:183], v3\n" \
    "s_waitcnt lgkmcnt(0)\n" \
    "v_smfmac_f32_16x16x64_bf16 v[48:51], v[44:47], v[184:191], v3\n" \
    "s_nop 7\n" \
    "v_add_f32 v48, v48, v50\n" \
    "v_add_f32 v49, v49, v51\n" \
    "v_cmp_u_f32 vcc, v48, v49\n" \
    "v_cvt_pk_bf16_f32 v52, v48, v49\n" \
    "s_cbranch_vccnz CVMFAIL_%=\n" \
    "CVMGO_%=:\n" \
    "buffer_store_dword v52, v4, s[40:43], s83 offen sc1\n" \
    "s_add_u32 s62, s62, 128\n" \
    "s_cmp_lg_u32 s63, 1\n" \
    "s_cbranch_scc1 CVNL1_%=\n" \
    "s_cmp_eq_u32 s72, 0\n" \
    "s_cbranch_scc1 CVFIRST_%=\n" \
    "CVFDONE_%=:\n" \
    "ds_read_b32 v89, v88 offset:20480\n" \
    "s_waitcnt lgkmcnt(0)\n" \
    "v_readfirstlane_b32 s46, v89\n" \
    "s_nop 1\n" \
    "s_xor_b32 s39, s45, s87\n" \
    "s_add_u32 s86, s86, 1\n" \
    "s_or_b32 s39, s39, s78\n" \
    "s_cselect_b32 s86, s86, 0\n" \
    "s_max_u32 s39, s86, 1\n" \
    "s_sub_u32 s39, s39, 1\n" \
    "s_min_u32 s39, s39, 2\n" \
    "s_mul_i32 s87, s39, 76\n" \
    "s_add_u32 s87, s87, s45\n" \
    "s_and_b32 s39, s46, 0xffff\n" \
    "s_lshr_b32 s47, s46, 16\n" \
    "s_add_u32 s39, s39, s47\n" \
    "s_add_u32 s87, s87, s39\n" \
    "v_mov_b32 v96, s87\n" \
    "v_mov_b32 v97, s46\n" \
    "s_mov_b64 exec, 1\n" \
    "buffer_store_dwordx2 v[96:97], off, s[40:43], s62 sc1\n" \
    "s_mov_b64 exec, -1\n" \
    "CVNL1_%=:\n" \
    "ds_write_b32 v6, v52 offset:8192\n" \
    "v_mov_b64 v[48:49], 0\n" \
    "v_mov_b64 v[50:51], 0\n" \
    "v_mov_b32 v100, s72\n" \
    "s_waitcnt lgkmcnt(0)\n" \
    "s_barrier\n" \
    "s_cmp_eq_u32 s72, 63\n" \
    "s_cbranch_scc1 CVLAST_%=\n" \
    "s_add_u32 s73, s52, s66\n" \
    "s_xor_b32 s52, s52, 0x84000\n" \
    "s_add_u32 s72, s72, 1\n" \
    "s_and_b32 s75, s72, 3\n" \
    "s_lshl_b32 s75, s75, 16\n" \
    "s_add_u32 s74, s54, s75\n" \
    "s_add_u32 s53, s74, s55\n" \
    "s_cmp_lt_u32 s72, 63\n" \
    "s_cselect_b32 s39, 0x100000, 0\n" \
    "s_add_u32 s58, s58, s39\n" \
    "s_xor_b32 s85, s75, 0x20000\n" \
    "s_lshl_b32 s75, s75, 1\n" \
    "s_add_u32 s83, s56, s75\n" \
    "s_cmp_eq_u32 s59, 0\n" \
    "s_cbranch_scc1 CVW0_%=\n" \
    "s_cmp_eq_u32 s63, 2\n" \
    "s_cbranch_scc1 CVADAPT_%=\n" \
    "s_sleep 6\n" \
    "s_cmp_eq_u32 s59, 2\n" \
    "s_cbranch_scc1 CVW2_%=\n" \
    "s_cmp_eq_u32 s59, 3\n" \
    "s_cbranch_scc1 CVW3_%=\n" \
    "s_add_u32 s39, s85, s54\n" \
    "buffer_store_dwordx2 v[84:85], v5, s[40:43], s39 offen sc1\n" \
    "s_lshl_b32 s39, s85, 1\n" \
    "s_add_u32 s39, s39, s56\n" \
    "buffer_store_dwordx4 v[84:87], v0, s[40:43], s39 offen sc1\n" \
    "s_branch CVWX_%=\n" \
    "CVW2_%=:\n" \
    "s_add_u32 s39, s52, s66\n" \
    "buffer_store_dwordx4 v[84:87], v0, s[40:43], s39 offen offset:0 sc1\n" \
    "buffer_store_dwordx4 v[84:87], v0, s[40:43], s39 offen offset:1024 sc1\n" \
    "buffer_store_dwordx4 v[84:87], v0, s[40:43], s39 offen offset:2048 sc1\n" \
    "buffer_store_dwordx4 v[84:87], v0, s[40:43], s39 offen offset:3072 sc1\n" \
    "s_branch CVWX_%=\n" \
    "CVW3_%=:\n" \
    "s_lshr_b32 s76, s57, 8\n" \
    "s_and_b32 s76, s76, 0x2000\n" \
    "s_xor_b32 s76, s76, s70\n" \
    "s_or_b32 s76, s76, s57\n" \
    "s_add_u32 s76, s76, s69\n" \
    "ds_read_b128 v[96:99], v0 offset:8192\n" \
    "s_mov_b64 exec, 1\n" \
    "buffer_store_dword v100, off, s[40:43], s60 sc1\n" \
    "s_mov_b64 exec, -1\n" \
    "s_waitcnt lgkmcnt(0)\n" \
    "buffer_store_dwordx4 v[96:99], v0, s[40:43], s76 offen sc1\n" \
    "s_add_u32 s57, s57, 0x20000\n" \
    "s_add_u32 s75, s72, 2\n" \
    "s_min_u32 s75, s75, 64\n" \
    "s_barrier\n" \
    "s_waitcnt vmcnt(3)\n" \
    "v_cmp_gt_i32 vcc, s75, v101\n" \
    "s_cbranch_vccz CVJOIN_%=\n" \
    "CVKP1_%=:\n" \
    "buffer_load_dword v96, v12, s[40:43], 0 offen sc1\n" \
    "s_add_u32 s79, s79, 1\n" \
    "s_cmp_ge_u32 s79, 0x20000\n" \
    "s_cbranch_scc1 CVKD1_%=\n" \
    "s_waitcnt vmcnt(0)\n" \
    "v_cmp_gt_i32 vcc, s75, v96\n" \
    "s_cbranch_vccz CVKD1_%=\n" \
    "s_sleep 2\n" \
    "s_branch CVKP1_%=\n" \
    "CVKD1_%=:\n" \
    "s_waitcnt vmcnt(0)\n" \
    "s_branch CVJOIN_%=\n" \
    "CVWX_%=:\n" \
    "s_waitcnt vmcnt(0)\n" \
    "s_barrier\n" \
    "s_branch CVJOIN_%=\n" \
    "CVW0_%=:\n" \
    "s_sleep 4\n" \
    "s_and_b32 s44, s46, 0xffff\n" \
    "s_add_u32 s44, s44, s45\n" \
    "s_sub_u32 s44, s44, 38\n" \
    "s_cmp_lg_u32 s63, 0\n" \
    "s_cbranch_scc1 CVLEADB_%=\n" \
    "CVGW2_%=:\n" \
    "s_sleep 1\n" \
    "buffer_load_dwordx2 v[96:97], off, s[40:43], s62 sc1\n" \
    "CVGC2_%=:\n" \
    "s_add_u32 s79, s79, 1\n" \
    "s_cmp_ge_u32 s79, 0x10000\n" \
    "s_cbranch_scc1 CVGD2_%=\n" \
    "s_waitcnt vmcnt(0)\n" \
    "v_cmp_eq_u32 vcc, -1, v97\n" \
    "s_cbranch_vccnz CVGW2_%=\n" \
    "v_readfirstlane_b32 s45, v96\n" \
    "v_readfirstlane_b32 s46, v97\n" \
    "CVGD2_%=:\n" \
    "s_nop 1\n" \
    "s_lshr_b32 s85, s46, 16\n" \
    "CVADONE_%=:\n" \
    "s_mov_b32 s78, 0\n" \
    "s_sub_u32 s45, s45, s85\n" \
    "s_sub_u32 s39, s44, s45\n" \
    "s_add_u32 s39, s39, 2048\n" \
    "s_cmp_lt_u32 s39, 0x4000\n" \
    "s_cselect_b32 s45, s45, s44\n" \
    "s_sub_u32 s45, s45, 76\n" \
    "CVGR3_%=:\n" \
    "s_add_u32 s45, s45, 76\n" \
    "s_sub_u32 s39, s45, s44\n" \
    "s_cmp_lt_i32 s39, 0\n" \
    "s_cbranch_scc1 CVGR3_%=\n" \
    "s_mov_b32 s44, s45\n" \
    "s_add_u32 s45, s44, s85\n" \
    "s_sub_u32 s75, s44, 16\n" \
    "s_branch CVWC4_%=\n" \
    "CVWS4_%=:\n" \
    "s_sleep 2\n" \
    "CVWC4_%=:\n" \
    "s_memrealtime s[26:27]\n" \
    "s_waitcnt lgkmcnt(0)\n" \
    "s_sub_u32 s39, s26, s75\n" \
    "s_cmp_lt_i32 s39, 0\n" \
    "s_cbranch_scc1 CVWS4_%=\n" \
    "s_memrealtime s[26:27]\n" \
    "s_sub_u32 s75, s44, 3\n" \
    "s_nop 1\n" \
    "CVWL4_%=:\n" \
    "s_memrealtime s[28:29]\n" \
    "s_waitcnt lgkmcnt(1)\n" \
    "s_sub_u32 s39, s26, s75\n" \
    "s_cmp_ge_i32 s39, 0\n" \
    "s_cbranch_scc1 CVWD4_%=\n" \
    "s_memrealtime s[26:27]\n" \
    "s_waitcnt lgkmcnt(1)\n" \
    "s_sub_u32 s39, s28, s75\n" \
    "s_cmp_ge_i32 s39, 0\n" \
    "s_cbranch_scc1 CVWD4_%=\n" \
    "s_memrealtime s[28:29]\n" \
    "s_waitcnt lgkmcnt(1)\n" \
    "s_sub_u32 s39, s26, s75\n" \
    "s_cmp_ge_i32 s39, 0\n" \
    "s_cbranch_scc1 CVWD4_%=\n" \
    "s_memrealtime s[26:27]\n" \
    "s_waitcnt lgkmcnt(1)\n" \
    "s_sub_u32 s39, s28, s75\n" \
    "s_cmp_ge_i32 s39, 0\n" \
    "s_cbranch_scc1 CVWD4_%=\n" \
    "s_memrealtime s[28:29]\n" \
    "s_waitcnt lgkmcnt(1)\n" \
    "s_sub_u32 s39, s26, s75\n" \
    "s_cmp_ge_i32 s39, 0\n" \
    "s_cbranch_scc1 CVWD4_%=\n" \
    "s_memrealtime s[26:27]\n" \
    "s_waitcnt lgkmcnt(1)\n" \
    "s_sub_u32 s39, s28, s75\n" \
    "s_cmp_ge_i32 s39, 0\n" \
    "s_cbranch_scc0 CVWL4_%=\n" \
    "CVWD4_%=:\n" \
    "s_min_u32 s39, s39, 3\n" \
    "s_sub_u32 s39, 3, s39\n" \
    "CVWR4_%=:\n" \
    "s_sub_u32 s39, s39, 1\n" \
    "s_cbranch_scc0 CVWR4_%=\n" \
    "buffer_load_dwordx4 v[64:67], v0, s[40:43], s73 offen offset:0 sc1\n" \
    "buffer_load_dwordx4 v[68:71], v0, s[40:43], s73 offen offset:1024 sc1\n" \
    "buffer_load_dwordx4 v[72:75], v0, s[40:43], s73 offen offset:2048 sc1\n" \
    "buffer_load_dwordx4 v[76:79], v0, s[40:43], s73 offen offset:3072 sc1\n" \
    "s_barrier\n" \
    "CVPCHK_%=:\n" \
    "s_waitcnt vmcnt(3)\n" \
    "v_mfma_f32_16x16x32_bf16 v[80:83], v[56:59], v[64:67], 0\n" \
    "v_max3_u32 v90, v64, v65, v66\n" \
    "s_waitcnt vmcnt(2)\n" \
    "v_mfma_f32_16x16x32_bf16 v[80:83], v[56:59], v[68:71], v[80:83]\n" \
    "v_max3_u32 v90, v90, v67, v68\n" \
    "v_max3_u32 v90, v90, v69, v70\n" \
    "s_waitcnt vmcnt(1)\n" \
    "v_mfma_f32_16x16x32_bf16 v[80:83], v[56:59], v[72:75], v[80:83]\n" \
    "v_max3_u32 v90, v90, v71, v72\n" \
    "v_max3_u32 v90, v90, v73, v74\n" \
    "s_waitcnt vmcnt(0)\n" \
    "v_mfma_f32_16x16x32_bf16 v[80:83], v[56:59], v[76:79], v[80:83]\n" \
    "v_max3_u32 v90, v90, v75, v76\n" \
    "v_max3_u32 v91, v77, v78, v79\n" \
    "v_max_u32 v90, v90, v91\n" \
    "v_cmp_eq_u32 vcc, -1, v90\n" \
    "s_nop 3\n" \
    "v_add_f32_dpp v80, v80, v80 row_ror:8 row_mask:0xf bank_mask:0xf\n" \
    "v_add_f32_dpp v81, v81, v81 row_ror:8 row_mask:0xf bank_mask:0xf\n" \
    "v_add_f32_dpp v82, v82, v82 row_ror:8 row_mask:0xf bank_mask:0xf\n" \
    "v_add_f32_dpp v83, v83, v83 row_ror:8 row_mask:0xf bank_mask:0xf\n" \
    "v_cvt_pk_bf16_f32 v90, v80, v81\n" \
    "v_cvt_pk_bf16_f32 v91, v82, v83\n" \
    "s_cbranch_vccnz CVPFAIL_%=\n" \
    "CVPGO_%=:\n" \
    "buffer_store_dwordx2 v[90:91], v5, s[40:43], s74 offen sc1\n" \
    "s_waitcnt lgkmcnt(0)\n" \
    "s_memrealtime s[26:27]\n" \
    "s_waitcnt lgkmcnt(0)\n" \
    "s_sub_u32 s84, s26, s44\n" \
    "s_add_u32 s47, s26, 4\n" \
    "s_sub_u32 s39, s47, s45\n" \
    "s_cmp_gt_i32 s39, 4\n" \
    "s_cbranch_scc1 CVLATE_%=\n" \
    "CVWAITB_%=:\n" \
    "s_memrealtime s[26:27]\n" \
    "s_sub_u32 s75, s45, 3\n" \
    "s_nop 1\n" \
    "CVWL5_%=:\n" \
    "s_memrealtime s[28:29]\n" \
    "s_waitcnt lgkmcnt(1)\n" \
    "s_sub_u32 s39, s26, s75\n" \
    "s_cmp_ge_i32 s39, 0\n" \
    "s_cbranch_scc1 CVWD5_%=\n" \
    "s_memrealtime s[26:27]\n" \
    "s_waitcnt lgkmcnt(1)\n" \
    "s_sub_u32 s39, s28, s75\n" \
    "s_cmp_ge_i32 s39, 0\n" \
    "s_cbranch_scc1 CVWD5_%=\n" \
    "s_memrealtime s[28:29]\n" \
    "s_waitcnt lgkmcnt(1)\n" \
    "s_sub_u32 s39, s26, s75\n" \
    "s_cmp_ge_i32 s39, 0\n" \
    "s_cbranch_scc1 CVWD5_%=\n" \
    "s_memrealtime s[26:27]\n" \
    "s_waitcnt lgkmcnt(1)\n" \
    "s_sub_u32 s39, s28, s75\n" \
    "s_cmp_ge_i32 s39, 0\n" \
    "s_cbranch_scc1 CVWD5_%=\n" \
    "s_memrealtime s[28:29]\n" \
    "s_waitcnt lgkmcnt(1)\n" \
    "s_sub_u32 s39, s26, s75\n" \
    "s_cmp_ge_i32 s39, 0\n" \
    "s_cbranch_scc1 CVWD5_%=\n" \
    "s_memrealtime s[26:27]\n" \
    "s_waitcnt lgkmcnt(1)\n" \
    "s_sub_u32 s39, s28, s75\n" \
    "s_cmp_ge_i32 s39, 0\n" \
    "s_cbranch_scc0 CVWL5_%=\n" \
    "CVWD5_%=:\n" \
    "s_min_u32 s39, s39, 3\n" \
    "s_sub_u32 s39, 3, s39\n" \
    "CVWR5_%=:\n" \
    "s_sub_u32 s39, s39, 1\n" \
    "s_cbranch_scc0 CVWR5_%=\n" \
    "CVJOIN_%=:\n" \
    "s_barrier\n" \
    "s_branch CVLOAD_%=\n" \
    "CVFIRST_%=:\n" \
    "s_sleep 2\n" \
    "buffer_load_dword v96, v8, s[40:43], s64 offen offset:2048 sc1\n" \
    "s_add_u32 s79, s79, 1\n" \
    "s_cmp_ge_u32 s79, 0x10000\n" \
    "s_cbranch_scc1 CVFDONE_%=\n" \
    "s_waitcnt vmcnt(0)\n" \
    "v_cmp_eq_u32 vcc, -1, v96\n" \
    "s_cbranch_vccnz CVFIRST_%=\n" \
    "v_subrev_u32 v96, s45, v96\n" \
    "s_nop 1\n" \
    "v_max_i32_dpp v96, v96, v96 quad_perm:[1,0,3,2] row_mask:0xf bank_mask:0xf\n" \
    "s_nop 1\n" \
    "v_max_i32_dpp v96, v96, v96 quad_perm:[2,3,0,1] row_mask:0xf bank_mask:0xf\n" \
    "s_nop 1\n" \
    "v_max_i32_dpp v96, v96, v96 row_ror:4 row_mask:0xf bank_mask:0xf\n" \
    "s_nop 1\n" \
    "v_max_i32_dpp v96, v96, v96 row_ror:8 row_mask:0xf bank_mask:0xf\n" \
    "v_mov_b32 v97, v96\n" \
    "s_nop 1\n" \
    "v_permlane16_swap_b32 v96, v97\n" \
    "v_max_i32 v96, v96, v97\n" \
    "v_mov_b32 v97, v96\n" \
    "s_nop 1\n" \
    "v_permlane32_swap_b32 v96, v97\n" \
    "v_max_i32 v96, v96, v97\n" \
    "s_nop 0\n" \
    "v_readfirstlane_b32 s39, v96\n" \
    "s_nop 1\n" \
    "s_add_u32 s45, s45, s39\n" \
    "s_add_u32 s45, s45, 20\n" \
    "s_mov_b32 s87, s45\n" \
    "s_branch CVFDONE_%=\n" \
    "CVX0_%=:\n" \
    "s_min_u32 s47, s84, 0x3ff\n" \
    "s_cmp_eq_u32 s78, 0\n" \
    "s_cselect_b32 s47, s47, 0\n" \
    "s_not_b32 s47, s47\n" \
    "v_mov_b32 v100, s47\n" \
    "s_mov_b64 exec, 1\n" \
    "buffer_store_dword v100, off, s[40:43], s65 sc1\n" \
    "s_mov_b64 exec, -1\n" \
    "s_branch CVXD_%=\n" \
    "CVX2_%=:\n" \
    "s_mov_b32 m0, 18432\n" \
    "s_nop 0\n" \
    "buffer_load_dword v7, s[40:43], s76 offen sc1 lds\n" \
    "s_branch CVXD_%=\n" \
    "CVLATE_%=:\n" \
    "s_sub_u32 s39, s47, s45\n" \
    "s_add_u32 s39, s39, 2048\n" \
    "s_cmp_lt_u32 s39, 0x4000\n" \
    "s_cselect_b32 s45, s45, s47\n" \
    "s_sub_u32 s45, s45, 76\n" \
    "CVGR6_%=:\n" \
    "s_add_u32 s45, s45, 76\n" \
    "s_sub_u32 s39, s45, s47\n" \
    "s_cmp_lt_i32 s39, 0\n" \
    "s_cbranch_scc1 CVGR6_%=\n" \
    "s_branch CVWAITB_%=\n" \
    "CVLEADB_%=:\n" \
    "s_lshr_b32 s85, s46, 16\n" \
    "s_mov_b32 s45, s87\n" \
    "s_sleep 8\n" \
    "s_branch CVADONE_%=\n" \
    "CVADAPT_%=:\n" \
    "buffer_load_dword v96, v8, s[40:43], s64 offen sc1\n" \
    "buffer_load_dword v97, v8, s[40:43], s64 offen offset:1024 sc1\n" \
    "s_sleep 6\n" \
    "s_add_u32 s39, s85, s54\n" \
    "buffer_store_dwordx2 v[84:85], v5, s[40:43], s39 offen sc1\n" \
    "s_lshl_b32 s39, s85, 1\n" \
    "s_add_u32 s39, s39, s56\n" \
    "buffer_store_dwordx4 v[84:87], v0, s[40:43], s39 offen sc1\n" \
    "s_waitcnt vmcnt(2)\n" \
    "v_and_b32 v96, 0xffff, v96\n" \
    "v_lshl_or_b32 v96, v97, 16, v96\n" \
    "s_nop 1\n" \
    "v_mov_b32_dpp v97, v96 quad_perm:[1,0,3,2] row_mask:0xf bank_mask:0xf\n" \
    "v_pk_min_u16 v96, v96, v97\n" \
    "s_nop 1\n" \
    "v_mov_b32_dpp v97, v96 quad_perm:[2,3,0,1] row_mask:0xf bank_mask:0xf\n" \
    "v_pk_min_u16 v96, v96, v97\n" \
    "s_nop 1\n" \
    "v_mov_b32_dpp v97, v96 row_ror:4 row_mask:0xf bank_mask:0xf\n" \
    "v_pk_min_u16 v96, v96, v97\n" \
    "s_nop 1\n" \
    "v_mov_b32_dpp v97, v96 row_ror:8 row_mask:0xf bank_mask:0xf\n" \
    "v_pk_min_u16 v96, v96, v97\n" \
    "v_mov_b32 v97, v96\n" \
    "s_nop 1\n" \
    "v_permlane16_swap_b32 v96, v97\n" \
    "v_pk_min_u16 v96, v96, v97\n" \
    "v_mov_b32 v97, v96\n" \
    "s_nop 1\n" \
    "v_permlane32_swap_b32 v96, v97\n" \
    "v_pk_min_u16 v96, v96, v97\n" \
    "s_nop 0\n" \
    "v_readfirstlane_b32 s47, v96\n" \
    "s_nop 1\n" \
    "s_not_b32 s47, s47\n" \
    "s_and_b32 s39, s47, 0xffff\n" \
    "s_lshl_b32 s39, s39, 3\n" \
    "s_cselect_b32 s39, s39, s77\n" \
    "s_add_u32 s75, s77, 40\n" \
    "s_min_u32 s39, s39, s75\n" \
    "s_sub_u32 s75, s77, s39\n" \
    "s_max_i32 s75, s75, 0\n" \
    "s_lshr_b32 s76, s75, 2\n" \
    "s_sub_u32 s75, s75, s76\n" \
    "s_add_u32 s77, s39, s75\n" \
    "s_lshr_b32 s39, s47, 16\n" \
    "s_lshl_b32 s39, s39, 3\n" \
    "s_cselect_b32 s39, s39, s82\n" \
    "s_add_u32 s75, s82, 40\n" \
    "s_min_u32 s39, s39, s75\n" \
    "s_sub_u32 s75, s82, s39\n" \
    "s_max_i32 s75, s75, 0\n" \
    "s_lshr_b32 s76, s75, 2\n" \
    "s_sub_u32 s75, s75, s76\n" \
    "s_add_u32 s82, s39, s75\n" \
    "s_lshr_b32 s39, s77, 3\n" \
    "s_lshr_b32 s75, s82, 3\n" \
    "s_pack_ll_b32_b16 s46, s39, s75\n" \
    "s_add_u32 s46, s46, 327687\n" \
    "v_mov_b32 v89, s46\n" \
    "ds_write_b32 v88, v89 offset:20480\n" \
    "s_waitcnt lgkmcnt(0)\n" \
    "s_branch CVWX_%=\n" \
    "CVPRETRY_%=:\n" \
    "buffer_load_dwordx4 v[64:67], v0, s[40:43], s73 offen offset:0 sc1\n" \
    "buffer_load_dwordx4 v[68:71], v0, s[40:43], s73 offen offset:1024 sc1\n" \
    "buffer_load_dwordx4 v[72:75], v0, s[40:43], s73 offen offset:2048 sc1\n" \
    "buffer_load_dwordx4 v[76:79], v0, s[40:43], s73 offen offset:3072 sc1\n" \
    "s_branch CVPCHK_%=\n" \
    "CVPFAIL_%=:\n" \
    "s_add_u32 s80, s80, 1\n" \
    "s_add_u32 s79, s79, 1\n" \
    "s_add_u32 s78, s78, 1\n" \
    "s_cmp_lt_u32 s78, 4\n" \
    "s_cbranch_scc1 CVPRETRY_%=\n" \
    "s_sleep 4\n" \
    "s_cmp_lt_u32 s79, 0x10000\n" \
    "s_cbranch_scc1 CVPRETRY_%=\n" \
    "s_branch CVPGO_%=\n" \
    "CVMFAIL_%=:\n" \
    "v_max3_u32 v90, v16, v17, v18\n" \
    "v_max3_u32 v90, v90, v19, v20\n" \
    "v_max3_u32 v90, v90, v21, v22\n" \
    "v_max3_u32 v90, v90, v23, v24\n" \
    "v_max3_u32 v90, v90, v25, v26\n" \
    "v_max3_u32 v90, v90, v27, v28\n" \
    "v_max3_u32 v90, v90, v29, v30\n" \
    "v_max3_u32 v90, v90, v31, v32\n" \
    "v_max3_u32 v90, v90, v33, v34\n" \
    "v_max3_u32 v90, v90, v35, v36\n" \
    "v_max3_u32 v90, v90, v37, v38\n" \
    "v_max3_u32 v90, v90, v39, v40\n" \
    "v_max3_u32 v90, v90, v41, v42\n" \
    "v_max3_u32 v90, v90, v43, v44\n" \
    "v_max3_u32 v90, v90, v45, v46\n" \
    "v_max_u32 v90, v90, v47\n" \
    "v_cmp_eq_u32 vcc, -1, v90\n" \
    "s_cbranch_vccz CVMGO_%=\n" \
    "s_add_u32 s80, s80, 0x100000\n" \
    "s_cmp_ge_u32 s80, 0xF0000000\n" \
    "s_cbranch_scc1 CVMCAP_%=\n" \
    "v_mov_b64 v[48:49], 0\n" \
    "v_mov_b64 v[50:51], 0\n" \
    "s_cmp_lg_u32 s59, 0\n" \
    "s_cbranch_scc1 CVJOIN_%=\n" \
    "s_memrealtime s[26:27]\n" \
    "s_waitcnt lgkmcnt(0)\n" \
    "s_add_u32 s45, s45, 76\n" \
    "s_sub_u32 s47, s26, 16\n" \
    "s_branch CVLATE_%=\n" \
    "CVMCAP_%=:\n" \
    "s_sub_u32 s80, s80, 0x100000\n" \
    "s_branch CVMGO_%=\n" \
    "CVLAST_%=:\n" \
    "s_cmp_lg_u32 s59, 3\n" \
    "s_cbranch_scc1 CVEND_%=\n" \
    "s_lshr_b32 s76, s57, 8\n" \
    "s_and_b32 s76, s76, 0x2000\n" \
    "s_xor_b32 s76, s76, s70\n" \
    "s_or_b32 s76, s76, s57\n" \
    "s_add_u32 s76, s76, s69\n" \
    "ds_read_b128 v[96:99], v0 offset:8192\n" \
    "v_mov_b32 v100, 64\n" \
    "s_waitcnt lgkmcnt(0)\n" \
    "buffer_store_dwordx4 v[96:99], v0, s[40:43], s76 offen sc1\n" \
    "s_waitcnt vmcnt(0)\n" \
    "s_mov_b64 exec, 1\n" \
    "buffer_store_dword v100, off, s[40:43], s60 sc1\n" \
    "s_mov_b64 exec, -1\n" \
    "CVEND_%=:\n" \
    "s_cmp_lg_u32 s63, 1\n" \
    "s_cbranch_scc1 CVEND2_%=\n" \
    "v_mov_b32 v96, s67\n" \
    "v_mov_b32 v97, s68\n" \
    "s_mov_b64 exec, 1\n" \
    "buffer_store_dwordx2 v[96:97], off, s[40:43], 0 sc1\n" \
    "s_mov_b64 exec, -1\n" \
    "CVEND2_%=:\n" \
    "s_waitcnt vmcnt(0)\n"

constexpr unsigned L_M = 0, L_X = 8192, L_SX = 12288, L_O = 16384, L_ZERO = 20480, L_WO = 24576, L_TOTAL = 159744;
DEVI unsigned xcc_id() { return (unsigned)__builtin_amdgcn_s_getreg(20 | (3 << 11)) & 7u; }
__host__ __device__ constexpr unsigned mpos(int r, int ks, int c) { return ks * 1024 + (r >> 1) * 256 + (((c >> 3) ^ (r >> 2)) * 128) + (r & 1) * 64 + (c & 7) * 8; }
#define C10(p, a) p #a "0", p #a "1", p #a "2", p #a "3", p #a "4", p #a "5", p #a "6", p #a "7", p #a "8", p #a "9"
#define C100(p, a) C10(p, a##0), C10(p, a##1), C10(p, a##2), C10(p, a##3), C10(p, a##4), C10(p, a##5), C10(p, a##6), C10(p, a##7), C10(p, a##8), C10(p, a##9)

constexpr unsigned ROW_P = 0, ROW_V = 32, ROW_M = 48, CM_W = 56 << 15, CM_R = 57 << 15, CM_CAL = 58 << 15;
DEVI unsigned xenc(unsigned x) { return ((x >> 1) & 1) | ((x >> 2) << 1) | ((x & 1) << 2); }
DEVI unsigned cpos(unsigned row, unsigned x) { return (row << 15) | ((xenc(x) ^ ((row >> 5) & 1) ^ ((row >> 2) & 4)) << 12); }
#define CHAIN_CLOBBERS "memory", "vcc", "scc", "v24", "v25", "v26", "v27", "v96", "v97", "v104", "v105", "v106", "v107", "v128", "v129", "v130", "v131", \
          "v192", "v193", "v194", "v195", "v196", "v197", "v198", "v199", C10("v", 20), C10("v", 21), C10("v", 22), C10("v", 23), C10("v", 24), "v250", "v251", "v252", "v253", "v254", "v255", \
          "a0", "a1", "a2", "a3", "a4", "a5", "a6", "a7", "a8", "a9", C10("a", 1), C10("a", 2), C10("a", 3), C10("a", 4), C10("a", 5), C10("a", 6), C10("a", 7), C10("a", 8), C10("a", 9), C100("a", 1), \
          C10("a", 20), C10("a", 21), C10("a", 22), C10("a", 23), C10("a", 24), "a250", "a251", "a252", "a253", "a254", "a255", \
          "s26", "s27", "s28", "s29", "s40", "s41", "s42", "s43", "s44", "s45", "s46", "s47", "s75", "s76", "s77", "s81", "s91"
DEVI unsigned chain_run(const u16* Wq, const u16* Wk, const u16* Wv, const u16* Wo, char* ws, unsigned comm_off, unsigned par, unsigned long long tag, int rot, int b7, int l, int w, int x, int slot32) {
    const int slot = slot32 & 15, h = (slot >> 1) * NX + x, half = slot & 1, j = slot * NX + x;
    const int n = l & 15, g = l >> 4, q4 = (l >> 2) & 3, p4 = l & 3, r8 = l & 7;
    const unsigned c12 = par << 12, cm = comm_off + (c12 & 0x2000);
    const int vb = (n >> 1) & 1, rr = 2 * (n >> 2) + (n & 1);
    const int rbase = 4 * (g >> 1) + (g & 1) + 16 * (q4 & 1) + 2 * (q4 >> 1);
    const unsigned kvf = CT_UNIT + (((l >> 2) & 1) * 64 + 2 * h + ((l >> 1) & 1)) * 128 + (l & 1) * 4;
    v16u V0; v8u V1; v16u S0; v8u S1;
    V0[0] = l * 16;
    V0[1] = w * 1024 + l * 16;
    V0[2] = L_M + mpos(rr, 0, 4 * g + 2 * vb);
    V0[3] = vb ? 0xEEEEu : 0x4444u;
    S0[0] = RFL((size_t)ws); S0[1] = RFL((size_t)ws >> 32);
    if (slot32 < 16) {
        const unsigned ppos = (half * 4 + w) * 1024 + g * 256 + ((n ^ ((g >> 1) << 3)) * 16);
        V0[4] = L_X + w * 1024 + (n >> 2) * 256 + (((4 * g) ^ (n >> 2)) * 16) + (n & 3) * 2;
        V0[6] = L_X + g * 256 + ((n ^ g) * 16);
        V0[5] = (L_X + g * 256 + (((n | 2) ^ g) * 16));
        V0[7] = L_O + (vb ? 0 : 2048) + (w >> 1) * 1024 + (2 * (w & 1) + (g >> 1)) * 256 + (4 * (n >> 2) + 2 * ((g & 1) ^ vb ^ 1) + (n & 1)) * 16;
        V0[8] = cpos(ROW_P + (ppos >> 9), (ppos >> 7) & 3) + h * 128 + (ppos & 127);
        V0[9] = (w >> 1) * 16384 + (w & 1) * 4096 + l * 16;
        V0[10] = ((w & 2) ^ 2) * 1024 + l * 16;
        V0[11] = w * 512 + (g * 8 + rr) * 16;
        V0[12] = vb ? 0u : 0xFF800000u;
        V0[13] = w * 256 + l * 4;
        V0[14] = kvf;
        V0[15] = 0;
        V1[0] = (4 * w + g) * 8192 + ((n ^ (g << 1)) * 16);
        V1[1] = (4 * w + g) * 8192 + ((n ^ ((g | 4) << 1)) * 16);
        const int fo = (l >> 5) | (((w >> 1) & 1) << 1);
        V1[2] = (2 * w + (l >> 5)) * 1024 + (((l & 31) ^ (fo << 1)) * 16);
        V1[3] = (2 * w + (l >> 5)) * 1024 + (((l & 31) ^ ((fo | 4) << 1)) * 16);
        const int fr = (rbase & 3) | ((rbase >> 2) & 4);
        V1[4] = L_WO + rbase * 256 + ((64 * w + 8 * p4) ^ (fr << 5));
        const int obase = 16 * (g & 1) + 8 * (g >> 1) + 4 * (q4 >> 1) + (q4 & 1);
        const int fq = (obase & 1) | ((obase >> 1) & 2) | ((obase >> 2) & 4);
        V1[5] = L_WO + obase * 512 + ((128 * w + 8 * p4) ^ (fq << 5));
        V1[6] = (vb ? L_X : L_ZERO) + w * 1024 + g * 256 + ((n ^ g) * 16); V1[7] = 0;
        const size_t pq = (size_t)(Wq + h * 128), pk = (size_t)(Wk + h * 128), po = (size_t)(Wo + (size_t)h * 65536 + half * 256);
        S0[2] = RFL(pq); S0[3] = RFL(pq >> 32); S0[4] = RFL(pk); S0[5] = RFL(pk >> 32); S0[6] = RFL(po); S0[7] = RFL(po >> 32);
        S0[8] = RFL(comm_off);
        S0[9] = RFL(WS_M0);
        S0[10] = RFL(cpos(ROW_M, x));
        S0[11] = RFL(cpos(ROW_V + (slot >> 2), x) + ((slot >> 1) & 1) * 2048);
        S0[12] = RFL(wspread(WS_KF + h * 16384 + w * 4096));
        S0[13] = RFL(wspread(WS_KM + h * 2048));
        S0[14] = RFL(w); S0[15] = RFL(half);
        S1[0] = RFL(w == 0 ? cm + CM_R + (j & 7) * 128 + (j >> 3) * 4 : CT_FLAG + (j * 4 + w) * 128);
        S1[1] = RFL(WS_CTL + 4096 + (j * 4 + w) * 32);
        S1[2] = RFL(cm + CM_W);
        S1[3] = rot; S1[4] = WS_SCR + j * 4096; S1[5] = 0; S1[6] = 0; S1[7] = 0;
        V0[15] = l == 0 ? CT_GO + b7 * 128 : CT_EPOCH;
    } else {
        V0[4] = w * 256 + l * 4;
        V0[5] = ((cpos(ROW_M + (j >> 5), (g >> 1) * 2 + (n >> 3)) ^ c12) - (ROW_M << 15)) + (j & 31) * 128 + (g & 1) * 64 + r8 * 8;
        V0[6] = l * 16 + w * 4;
        V0[7] = l * 128;
        V0[8] = (l & 7) * 128 + (l >> 3) * 4;
        V0[9] = 0x3F80u << (16 * (l & 1));
        {
            const int row = 8 * w + (l >> 3), sp = ((l >> 1) & 3) ^ (((row >> 1) & 1) | ((row >> 3) & 2));
            V0[10] = row * 8192 + sp * 64 + (l & 1) * 16;
            V0[11] = L_WO + rbase * 128 + ((w ^ (((rbase >> 1) & 1) | ((rbase >> 3) & 2))) * 32) + p4 * 8;
        }
        V0[12] = w == 1 ? WS_SCR : kvf; V0[13] = 0; V0[14] = 0; V0[15] = 0;
        V1 = v8u{V0[0], V0[0], V0[0], V0[0], V0[0], V0[0], V0[0], V0[0]};
        const size_t pv = (size_t)(Wv + h * 128 + half * 16);
        S0[2] = RFL(pv); S0[3] = RFL(pv >> 32);
        S0[4] = RFL(cpos(ROW_P + slot, x) ^ c12);
        S0[5] = RFL(WS_M0);
        S0[6] = RFL(comm_off + (ROW_M << 15));
        S0[7] = RFL((cpos(ROW_M, x) ^ c12) - (ROW_M << 15));
        S0[8] = RFL(comm_off + (cpos(ROW_V + (slot >> 2), x) ^ c12) + ((slot >> 1) & 1) * 2048 + half * 1024);
        S0[9] = RFL(wspread(WS_VM + h * 2048 + half * 1024));
        S0[10] = RFL(wspread(WS_KF + 0x80000 + h * 16384 + half * 8192));
        S0[11] = RFL(w);
        S0[12] = RFL(CT_FLAG + (j * 4) * 128);
        S0[13] = RFL(WS_CTL + 4096 + (256 + j * 4 + w) * 32);
        S0[14] = RFL(cm + CM_W);
        S0[15] = RFL((j == 0 && w < 2) ? w + 1 : 0);
        S1[0] = RFL(cm + CM_R);
        S1[1] = RFL(cm + CM_R + 1024 + (j & 7) * 128 + (j >> 3) * 4);
        S1[2] = comm_off; S1[3] = (unsigned)tag; S1[4] = (unsigned)(tag >> 32); S1[5] = 0; S1[6] = 0; S1[7] = 0;
        V0[15] = l == 0 ? CT_GO + b7 * 128 : CT_EPOCH;
    }
#pragma unroll
    for (int i = 0; i < 16; ++i) S0[i] = RFL(S0[i]);
#pragma unroll
    for (int i = 0; i < 7; ++i) S1[i] = RFL(S1[i]);
    S1[5] = RFL(comm_off + (2u << 20)); S1[6] = RFL(c12 & 0x2000);
    S1[7] = RFL(slot32 >> 4);
    asm volatile("s_cmp_lg_u32 s71, 0\n s_cbranch_scc1 CHAIN_VALUE_%=\n" SCORE_ASM "s_endpgm\n CHAIN_VALUE_%=:\n" VALUE_ASM "s_endpgm\n CSREDO_%=:\n"
        : "+{v[0:15]}"(V0), "+{v[16:23]}"(V1), "+{s[48:63]}"(S0), "+{s[64:71]}"(S1) :: CHAIN_CLOBBERS);
    return S1[3];
}

DEVI float wsum(float v) {
    v += __builtin_bit_cast(float, __builtin_amdgcn_update_dpp(0, __builtin_bit_cast(int, v), 0xB1, 0xf, 0xf, true));
    v += __builtin_bit_cast(float, __builtin_amdgcn_update_dpp(0, __builtin_bit_cast(int, v), 0x4E, 0xf, 0xf, true));
    v += __builtin_bit_cast(float, __builtin_amdgcn_update_dpp(0, __builtin_bit_cast(int, v), 0x124, 0xf, 0xf, true));
    v += __builtin_bit_cast(float, __builtin_amdgcn_update_dpp(0, __builtin_bit_cast(int, v), 0x128, 0xf, 0xf, true));
    return x32_add(x16_add(v));
}

struct Args { const u16 *Wq, *Wk, *Wv, *wg, *wu, *Wo, *wd, *x, *mem0; u16* out; char* ws; };
typedef __attribute__((address_space(4))) const Args* KA;

__global__ void __launch_bounds__(256, 1) k_main(Args args_) {
    KA ka = (KA)__builtin_amdgcn_kernarg_segment_ptr();
    const int b = blockIdx.x, slot = b >> 3, w = __builtin_amdgcn_readfirstlane(threadIdx.x >> 6);
    int rot = (xcc_id() - b) & 7;
    unsigned long long tag = 0;
    unsigned near = 0;
    if (slot >= 16) {
        char* const ws = ka->ws; const u16* const x = ka->x;
        const unsigned comm_off = comm_offset(ws);
        const int tid = threadIdx.x, l = tid & 63, bb = b - 128;
        const __amdgpu_buffer_rsrc_t rS = __builtin_amdgcn_make_buffer_rsrc(ws, 0, 0x10000000, 0x00020000);
        const unsigned xcc = xcc_id();
        v4u xv[4];
#pragma unroll
        for (int r = 0; r < 4; ++r) xv[r] = *(const GAS v4u*)(x + (bb * 16 + w * 4 + r) * 512 + l * 8);
        const unsigned long long e = __hip_atomic_load((GAS unsigned long long*)(ws + CT_EPOCH), RLX_AGENT);
        const v4u sent = {SENT, SENT, SENT, SENT}, zero = {0, 0, 0, 0};
        if (bb * 16384 < CM_CAL) {
#pragma unroll
            for (int k = 0; k < 4; ++k) st16(sent, rS, tid * 16, comm_off + bb * 16384 + k * 4096);
        } else if (bb < CM_CAL / 16384 + 2) {
            const unsigned o = (bb - CM_CAL / 16384) * 4096 + tid * 16; const int ks = o >> 10, gr = (o >> 8) & 3, r = 2 * gr + ((o >> 6) & 1), c = ((((o >> 7) & 1) ^ (gr >> 1)) << 3) + ((o >> 3) & 7);
            v4u v;
#pragma unroll
            for (int i = 0; i < 2; ++i) {
                const u16* src = ka->mem0 + r * 512 + 64 * ks + c + i;
                v[2 * i] = (unsigned)src[0] | ((unsigned)src[16] << 16); v[2 * i + 1] = (unsigned)src[32] | ((unsigned)src[48] << 16);
            }
            st16(v, rS, (o & 4095) + (o >> 12) * 32768, WS_M0);
        }
        if (tid < 6) st16(zero, rS, CT_FLAG + (bb * 6 + tid) * 128, 0);
#pragma unroll 1
        for (int r = 0; r < 4; ++r) {
            const v4u xr = r == 0 ? xv[0] : (r == 1 ? xv[1] : (r == 2 ? xv[2] : xv[3]));
            float f[8];
#pragma unroll
            for (int i = 0; i < 4; ++i) { f[2 * i] = __uint_as_float(xr[i] << 16); f[2 * i + 1] = __uint_as_float(xr[i] & 0xffff0000u); }
            const float mean = wsum(((f[0] + f[1]) + (f[2] + f[3])) + ((f[4] + f[5]) + (f[6] + f[7]))) * (1.f / 512.f);
            float q = 0;
#pragma unroll
            for (int i = 0; i < 8; ++i) q += (f[i] - mean) * (f[i] - mean);
            const float mb = rb(mean), is = 1.f / rb(sqrtf(rb(rb(wsum(q) * (1.f / 512.f)) + 1e-5f)));
#pragma unroll
            for (int hh = 0; hh < 2; ++hh)
                *(LAS v2u*)(size_t)(w * 4096 + (l >> 2) * 256 + ((l & 1) * 2 + hh) * 64 + r * 16 + ((l >> 1) & 1) * 8) =
                    v2u{pk2(rb(f[4 * hh] - mb) * is, rb(f[4 * hh + 1] - mb) * is), pk2(rb(f[4 * hh + 2] - mb) * is, rb(f[4 * hh + 3] - mb) * is)};
        }
#pragma unroll
        for (int k = 0; k < 4; ++k) st16(lds128(w * 4096 + k * 1024 + l * 16), rS, (l >> 2) * 256 + (l & 3) * 16, WS_XN + bb * 16384 + k * 4096 + w * 64);
        tag = ((unsigned long long)RFL(e >> 32) << 32 | RFL(e)) & ~0xFFull; tag += 0x100;
        asm volatile("s_waitcnt vmcnt(0)" ::: "memory");
#pragma unroll 1
        for (int k = 0; k < 2; ++k) {
            const unsigned col = (w * 2 + k + (bb >> 3)) & 7;
            const unsigned c0 = clk();
            if (l < 8) __builtin_amdgcn_raw_buffer_store_b128(sent, rS, l * 16, comm_off + CM_CAL + (bb >> 5) * 32768 + col * 4096 + (bb & 31) * 128, 16);
            asm volatile("s_waitcnt vmcnt(0)" ::: "memory");
            const unsigned c = clk() - c0;
            if (l == 0) *(LAS unsigned*)(size_t)(16384 + col * 4) = c * 8 + col;
        }
        asm volatile("s_waitcnt lgkmcnt(0)\n\ts_barrier" ::: "memory");
        if (w == 0) {
            const v4u d0 = lds128(16384), d1 = lds128(16400);
            const unsigned best = min(min(min(d0.x, d0.y), min(d0.z, d0.w)), min(min(d1.x, d1.y), min(d1.z, d1.w))) & 7;
            if (l == 0) __hip_atomic_store((GAS unsigned long long*)(ws + CT_INIT + bb * 8), tag | (xcc << 3) | (best ^ ((cpos(CM_CAL >> 15, xcc) >> 12) & 7)), RLX_AGENT);
            if (bb < 8) {
                unsigned long long v0, v1; unsigned n = 0;
                for (;;) {
                    v0 = __hip_atomic_load((GAS unsigned long long*)(ws + CT_INIT + l * 8), RLX_AGENT); v1 = __hip_atomic_load((GAS unsigned long long*)(ws + CT_INIT + 512 + l * 8), RLX_AGENT);
                    if (__all((v0 & ~0xFFull) == tag && (v1 & ~0xFFull) == tag) || ++n > POLL_CAP) break;
                    __builtin_amdgcn_s_sleep(4);
                }
                const int n0 = __popcll(__ballot(v0 & 1)) + __popcll(__ballot(v1 & 1)), n1 = __popcll(__ballot(v0 & 2)) + __popcll(__ballot(v1 & 2)), n2 = __popcll(__ballot(v0 & 4)) + __popcll(__ballot(v1 & 4));
                if (l < 8) __hip_atomic_store((GAS unsigned long long*)(ws + CT_GO + l * 128), tag | (RFL(v0) & 56u) | (((n0 > 64 ? 1u : 0u) | (n1 > 64 ? 2u : 0u) | (n2 > 64 ? 4u : 0u)) ^ CFLIP), RLX_AGENT);
            }
        }
        const unsigned go = wait_go(ws, b & 7, tag);
        rot = go >> 3; near = go & 7;
        asm volatile("s_barrier" ::: "memory");
    }
    unsigned p13 = 0; int xx, b_ = b, w_ = w;
#pragma unroll 1
    for (;;) {
        asm volatile("" : "+s"(ka), "+s"(rot), "+s"(b_), "+s"(w_), "+s"(tag), "+s"(near));
        int l; asm volatile("v_mbcnt_lo_u32_b32 %0, -1, 0\n\tv_mbcnt_hi_u32_b32 %0, -1, %0" : "=v"(l));
        __builtin_assume(l >= 0 && l < 64);
        const int b = b_ & 255, w = w_ & 3, slot = b >> 3;
        char* const ws = ka->ws;
        const unsigned comm_off = comm_offset(ws);
        char* const wide = ws + comm_off + (2u << 20);
        xx = (b + rot) & 7;
        unsigned go;
        if (xx < NX) go = chain_run(ka->Wq, ka->Wk, ka->Wv, ka->Wo, ws, comm_off, near, tag, rot, b & 7, l, w, xx, slot);
        else {
            const int type = slot >> 3, quad = (xx - NX) * 4 + ((slot >> 1) & 3), par = slot & 1;
            go = rot * 8 + near;
            if (type == 1) go = phaseA_run<1>((size_t)ka->Wv, (size_t)ka->Wv, ws, wide, go, b & 7, l, w, 1, quad, par);
            else if (type == 3) { if (quad < 14 && !par) go = phaseA_run<3>((size_t)ka->wg, (size_t)ka->wu - 64, ws, wide, go, b & 7, l, w, 3, quad, par); }
            else if (type == 0 || !par) { const size_t wb = type ? (size_t)ka->Wq : (size_t)ka->Wk; go = phaseA_run<0>(wb, wb, ws, wide, go, b & 7, l, w, type, quad, par); }
        }
        p13 = (go & 2) << 12;
        if ((int)(go >> 3) == rot) break;
        rot = go >> 3;
        asm volatile("s_waitcnt vmcnt(0) lgkmcnt(0)\n\ts_barrier" ::: "memory");
    }
    asm volatile("" : "+s"(ka), "+s"(b_), "+s"(w_));
    char* const ws = ka->ws;
    int l; asm volatile("v_mbcnt_lo_u32_b32 %0, -1, 0\n\tv_mbcnt_hi_u32_b32 %0, -1, %0" : "=v"(l));
    __builtin_assume(l >= 0 && l < 64);
    if (b_ & 8) phaseB_run(ka->Wo, ka->wd, ka->x, ka->out, ws, ws + comm_offset(ws) + (2u << 20), p13, l, w_ & 3, ((b_ >> 6) + 2) & 3, 4, (xx - NX) * 4 + ((b_ >> 4) & 3));
}

extern "C" void kernel_launch(void* const* d_in, const int* in_sizes, int n_in, void* d_out, int out_size, void* d_ws, size_t ws_size, hipStream_t st) {
    Args a;
    a.x = (const u16*)d_in[0]; a.mem0 = (const u16*)d_in[1]; a.Wq = (const u16*)d_in[2]; a.Wk = (const u16*)d_in[3]; a.Wv = (const u16*)d_in[4]; a.Wo = (const u16*)d_in[5];
    a.wg = (const u16*)d_in[8]; a.wu = (const u16*)d_in[9]; a.wd = (const u16*)d_in[10]; a.out = (u16*)d_out; a.ws = (char*)d_ws;
    (void)hipFuncSetAttribute((const void*)k_main, hipFuncAttributeMaxDynamicSharedMemorySize, L_TOTAL);
    k_main<<<256, 256, L_TOTAL, st>>>(a);
}
```
